```python
import torch

def _softmax(x, axis=-1):
    return torch.softmax(x, dim=axis)
def _gelu(x):
    return torch.nn.functional.gelu(x, approximate="tanh")
def _silu(x):
    return torch.nn.functional.silu(x)
def _sigmoid(x):
    return torch.sigmoid(x)
def _layernorm(x, axis=-1, eps=1e-5):
    m = x.mean(dim=axis, keepdim=True)
    v = x.var(dim=axis, keepdim=True, unbiased=False)
    return (x - m) / torch.sqrt(v + eps)
def _scatter_add(tgt, idx, src, axis=0):
    return tgt.clone().index_add_(axis, idx, src)
def _segment_sum(src, seg, n):
    out = torch.zeros((n,) + src.shape[1:], dtype=src.dtype)
    return out.index_add_(0, seg, src)
def _pad(x, pw):
    flat = tuple(v for pair in reversed(pw) for v in pair)
    return torch.nn.functional.pad(x, flat)

kernel_name = 'recurrent_scan__spectral_fnet__moe_arch_mod__prompt'

def setup_inputs():

    _w = (lambda a: (lambda t: t.to(torch.bfloat16) if t.is_floating_point() else t)(torch.as_tensor(a)))
    def _A_setup():
        B, S, D = 1, 2048, 1024
        ns = torch.arange(S)[:, None] * torch.arange(S)[None, :]
        nd = torch.arange(D)[:, None] * torch.arange(D)[None, :]
        Ws_re = torch.cos(-2.0 * torch.pi * ns / S).to(float)
        Ws_im = torch.sin(-2.0 * torch.pi * ns / S).to(float)
        Wd_re = torch.cos(-2.0 * torch.pi * nd / D).to(float)
        Wd_im = torch.sin(-2.0 * torch.pi * nd / D).to(float)
        return {
            "x": _w((lambda *s: torch.randn(*s, dtype=torch.bfloat16) * 0.05)(B, S, D)),
            "dft_seq_re": _w(Ws_re),
            "dft_seq_im": _w(Ws_im),
            "dft_hid_re": _w(Wd_re),
            "dft_hid_im": _w(Wd_im),
        }
    _A_in = _A_setup()

    def _B_setup():
        B, S, D, F, C = 1, 2048, 1024, 3584, 1
        return {
            "x": _w((lambda *s: torch.randn(*s, dtype=torch.bfloat16) * 0.05)(B, S, D)),
            "w_r": _w((lambda *s: torch.randn(*s, dtype=torch.bfloat16) * 0.05)(D)),
            "w_gate": _w((lambda *s: torch.randn(*s, dtype=torch.bfloat16) * 0.05)(D, F)),
            "w_up": _w((lambda *s: torch.randn(*s, dtype=torch.bfloat16) * 0.05)(D, F)),
            "w_down": _w((lambda *s: torch.randn(*s, dtype=torch.bfloat16) * 0.05)(F, D)),
        }
    _B_in = _B_setup()

    return {
        "A_x": _A_in["x"],
        "A_dft_seq_re": _A_in["dft_seq_re"],
        "A_dft_seq_im": _A_in["dft_seq_im"],
        "A_dft_hid_re": _A_in["dft_hid_re"],
        "A_dft_hid_im": _A_in["dft_hid_im"],
        "B_x": _B_in["x"],
        "B_w_r": _B_in["w_r"],
        "B_w_gate": _B_in["w_gate"],
        "B_w_up": _B_in["w_up"],
        "B_w_down": _B_in["w_down"],
    }

def reference(A_x, A_dft_seq_re, A_dft_seq_im, A_dft_hid_re, A_dft_hid_im, B_x, B_w_r, B_w_gate, B_w_up, B_w_down):
    def _A_ref(x, dft_seq_re, dft_seq_im, dft_hid_re, dft_hid_im):
        a = torch.einsum("bsd,ed->bse", x, dft_hid_re)
        b = torch.einsum("bsd,ed->bse", x, dft_hid_im)
        y = torch.einsum("ts,bse->bte", dft_seq_re, a) - torch.einsum("ts,bse->bte", dft_seq_im, b)
        return y

    def _B_ref(x, w_r, w_gate, w_up, w_down):
        B, S, D = x.shape
        C = 1
        r = x @ w_r
        idx = torch.argsort(-r, axis=-1)[:, :C]
        idx_e = (lambda x, n, axis: torch.repeat_interleave(x, n, dim=axis))(idx.reshape(B, C, 1), D, axis=2)
        x_sel = (lambda x, i, axis: torch.take_along_dim(x, i, dim=axis))(x, idx_e, axis=1)
        g = _silu(x_sel @ w_gate)
        u = x_sel @ w_up
        y = (g * u) @ w_down
        r_sel = _sigmoid((lambda x, i, axis: torch.take_along_dim(x, i, dim=axis))(r, idx, axis=1))
        y = y * r_sel.reshape(B, C, 1)
        b_off = (torch.arange(B) * S).reshape(B, 1)
        flat_idx = (idx + b_off).reshape(B * C)
        zero = (x * 0.0).reshape(B * S, D)
        upd = _scatter_add(zero, flat_idx, y.reshape(B * C, D), axis=0)
        return x + upd.reshape(B, S, D)

    x = A_x
    xn = _layernorm(x, axis=-1)
    s = _A_ref(xn, A_dft_seq_re, A_dft_seq_im, A_dft_hid_re, A_dft_hid_im)
    g = _silu(_B_ref(xn, B_w_r, B_w_gate, B_w_up, B_w_down))
    return x + s * g
```

```cpp
#define CODE_LINES 278
#include <hip/hip_runtime.h>
#include <hip/hip_bf16.h>
#include <stdint.h>
#include <utility>

typedef __attribute__((ext_vector_type(8))) short bf16x8;
typedef __attribute__((ext_vector_type(4))) float f32x4;
typedef __attribute__((ext_vector_type(2))) float f32x2;
typedef __attribute__((ext_vector_type(4))) unsigned u32x4;
typedef __attribute__((ext_vector_type(2))) unsigned u32x2;
typedef __attribute__((ext_vector_type(2))) __bf16 bf16x2;
typedef unsigned short u16;
typedef unsigned long long u64;
typedef __amdgpu_buffer_rsrc_t rsrc_t;
#define LAS __attribute__((address_space(3)))

constexpr int S = 2048, D = 1024, F = 3584;
constexpr int UB = 16384;
constexpr int SLOT = 3 * UB;
constexpr int L_MISC = 9 * UB;
constexpr int L_UPD = L_MISC + 256;
constexpr int L_MB = L_MISC + 512;
constexpr int L_CS = L_MB + 1024;
constexpr int L_CMB = L_CS + 1024;
constexpr int L_DBG = L_CMB + 2048;
constexpr int LDS_BYTES = L_DBG + 512;
constexpr int L_ST = 6 * UB;
constexpr int NBAR = 17 + 14 + 3;
constexpr int SC1 = 16, NT = 2;
constexpr int OOB = 0x7ffffff0;

constexpr unsigned WS_EPOCH = 0;
constexpr unsigned WS_STAT = 4096;
constexpr unsigned WS_AMAX = WS_STAT + S * 8;
constexpr unsigned WS_FLAG1 = WS_AMAX + 4096;
constexpr unsigned WS_GU = WS_FLAG1 + 32768;
constexpr unsigned WS_PART = WS_GU + 4 * 2 * F * 8;
constexpr unsigned WS_XCH = WS_PART + 32 * D * 8;
constexpr int XW = 14336;
constexpr int XB = 4 * XW;
constexpr unsigned ABT_ST = 128 * S * 2;
constexpr unsigned WS_ABT = WS_XCH + 256 * XB;
constexpr unsigned WS_END = WS_ABT + 16 * ABT_ST;

#define STAMP(i)
#define CBAR() BAR()
#define STAMP_DECL
#define STAMP_ARG
#define STAMP_PASS
#define STAMP_OUT()

__device__ __forceinline__ float lo2f(unsigned v) { return __uint_as_float(v << 16); }
__device__ __forceinline__ float hi2f(unsigned v) { return __uint_as_float(v & 0xffff0000u); }
__device__ __forceinline__ unsigned pack2(float a, float b) {
  return __builtin_bit_cast(unsigned, __builtin_convertvector(f32x2{a, b}, bf16x2));
}
__device__ __forceinline__ float rbf(float f) { return lo2f(pack2(f, f)); }
__device__ __forceinline__ unsigned rfl(unsigned v) { return __builtin_amdgcn_readfirstlane(v); }
__device__ __forceinline__ rsrc_t mk_rsrc(const void* p, unsigned bytes) {
  return __builtin_amdgcn_make_buffer_rsrc((void*)p, 0, bytes, 0x00020000);
}
#define DPP_ADD(v, ctrl, rm, bc) v += __builtin_bit_cast(float, __builtin_amdgcn_update_dpp(0, __builtin_bit_cast(int, v), ctrl, rm, 0xf, bc))
#define DPP_MAXU(v, ctrl, rm, bc) v = max(v, (unsigned)__builtin_amdgcn_update_dpp(0, (int)v, ctrl, rm, 0xf, bc))
__device__ __forceinline__ unsigned wave_maxu(unsigned v) {
  DPP_MAXU(v, 0x111, 0xf, true); DPP_MAXU(v, 0x112, 0xf, true); DPP_MAXU(v, 0x114, 0xf, true); DPP_MAXU(v, 0x118, 0xf, true);
  DPP_MAXU(v, 0x142, 0xa, false); DPP_MAXU(v, 0x143, 0xc, false);
  return (unsigned)__builtin_amdgcn_readlane((int)v, 63);
}
template <int N> __device__ __forceinline__ void wait_vm() { asm volatile("s_waitcnt vmcnt(%0)" :: "n"(N) : "memory"); }
#define WAIT_LGKM0() do { asm volatile("" ::: "memory"); __builtin_amdgcn_s_waitcnt(0xc07f); asm volatile("" ::: "memory"); } while (0)
#define BAR() do { asm volatile("" ::: "memory"); __builtin_amdgcn_s_barrier(); asm volatile("" ::: "memory"); } while (0)
#define MFMA(a, b, c) __builtin_amdgcn_mfma_f32_16x16x32_bf16(a, b, c, 0, 0, 0)
#define BF8(v) __builtin_bit_cast(bf16x8, v)
#define PIN(a) asm volatile("" : "+v"(a))
#define PIN4(a, b, c, d) asm volatile("" : "+v"(a), "+v"(b), "+v"(c), "+v"(d))
constexpr int SPIN_CAP = 1 << 20;
__device__ __forceinline__ unsigned vm_now() { const unsigned r = __builtin_amdgcn_s_getreg(7 | (31 << 11)); return (r & 15u) | ((r >> 18) & 0x30u); }
__device__ __forceinline__ unsigned mk_tag(unsigned epoch) { return rfl(epoch) * 0x9E3779B1u + 0x7F4A7C15u; }
__device__ __forceinline__ float silu_fast(float z) { return z * __builtin_amdgcn_rcpf(1.0f + __expf(-z)); }
__device__ __forceinline__ u32x4 pack8(const f32x4& a, const f32x4& c) {
  return u32x4{pack2(a[0], a[1]), pack2(a[2], a[3]), pack2(c[0], c[1]), pack2(c[2], c[3])};
}
__device__ __forceinline__ unsigned ln_pair(unsigned x, float nmb, float rden) {
  const unsigned d = pack2(lo2f(x) + nmb, hi2f(x) + nmb);
  return pack2(lo2f(d) * rden, hi2f(d) * rden);
}
#define ADD2_DPP(a, b, mod) asm volatile("s_nop 1\n\tv_add_f32_dpp %0, %0, %0 " mod " row_mask:0xf bank_mask:0xf\n\tv_add_f32_dpp %1, %1, %1 " mod " row_mask:0xf bank_mask:0xf" : "+v"(a), "+v"(b))
#define ALLEQ(a, t) (__builtin_amdgcn_ballot_w64((a) == (t)))
__device__ __forceinline__ unsigned silu_pair(unsigned n) { return pack2(silu_fast(lo2f(n)), silu_fast(hi2f(n))); }
__device__ __forceinline__ int lane_now() { int l; asm volatile("v_mbcnt_lo_u32_b32 %0, -1, 0\n\tv_mbcnt_hi_u32_b32 %0, -1, %0" : "=v"(l)); return l; }

#define PRE_H 1
#define CT2_AT 18
#define CT3_AT 28
#define NEED_TO 23
#define SC_AT 3
#define PT_AT 3
#define XN_AT 17
#define SET2_AT 16
#define AM_AT 12
#define AT_AT 14
#define Q0_AT 14
#define Q1_AT 15
#define CGU_AT 19
#define GG_AT 21
#define GD_AT 23
#define GE_AT 24
constexpr int XE_AT = AT_AT + 1, Q2_AT = 17;
#define H_SCHED      {2,2,3,3,2,2,2,2,2,2,2,2,2,2,0,0,0,0,0,0,0,0,0,0,0,0,0,0,0,0,0,0,0,0,0}
#define V_SCHED      {0,0,0,0,0,0,0,0,0,0,0,0,0,0,0,0,0,0,8,0,0,0,0,0,0,0,0,0,0,0,0,0,0,0,0}
#define W_SCHED      {0,0,0,0,3,0,0,3,0,0,3,0,0,4,3,0,0,0,0,0,0,0,7,0,0,0,0,0,0,0,0,0,0,0,0}
#define T_SCHED      {0,0,4,4,4,4,4,4,4,4,4,4,4,4,4,0,0,0,0,0,0,0,0,0,0,0,0,0,0,0,0,0,0,0,0}
#define U_SCHED      {0,0,0,0,0,0,0,0,0,0,0,0,0,0,0,0,0,0,0,0,44,0,0,0,0,32,0,0,0,0,0,0,0,0,0}
#define GF_N 2
#define RX_FROM 6
#define KROT(tp1) ((((tp1) & 12) + (tq1 & 8) + ((tq1 ^ (tp1)) & 1)) & 15)
typedef const __attribute__((address_space(4))) u64* karg_t;

#define DMA4(POL, r, v0, v1, v2, v3, so, dst) do { \
    asm volatile("s_mov_b32 m0, %6\n\ts_nop 0\n\tbuffer_load_dwordx4 %0, %4, %5 offen" POL " lds\n\t" \
                 "buffer_load_dwordx4 %1, %4, %5 offen offset:1024" POL " lds\n\tbuffer_load_dwordx4 %2, %4, %5 offen offset:2048" POL " lds\n\t" \
                 "buffer_load_dwordx4 %3, %4, %5 offen offset:3072" POL " lds" \
                 :: "v"(v0), "v"(v1), "v"(v2), "v"(v3), "s"(r), "s"(so), "s"(dst) : "memory"); } while (0)
#define LD4Z(POL, d, r, vo) do { asm volatile("buffer_load_dwordx4 %0, %1, %2, 0 offen" POL : "=v"(d) : "v"(vo), "s"(r) : "memory"); ++ops; } while (0)
#define TOUCH(POL, r, vo) do { asm volatile("buffer_load_dword %0, %1, %2, 0 offen" POL : "+v"(tch) : "v"(vo), "s"(r) : "memory"); ++ops; } while (0)
#define ST4(d, vo) asm volatile("buffer_store_dwordx4 %0, %1, %2, 0 offen sc1\n\ts_nop 1" :: "v"(d), "v"(vo), "s"(rws) : "memory")
#define ST1(d, vo) asm volatile("buffer_store_dword %0, %1, %2, 0 offen sc1" :: "v"(d), "v"(vo), "s"(rws) : "memory")
struct Cnt {
  int x, y0, y3;
  __device__ __forceinline__ void operator++() { ++x; }
  __device__ __forceinline__ void operator+=(int n) { x += n; }
};
__device__ __forceinline__ void wait_n(int n) { asm volatile("s_waitcnt vmcnt(%0)" :: "n"(n > 63 ? 63 : n) : "memory"); }
__device__ __forceinline__ void wait_mark(Cnt ops, Cnt m, int v) {
  const int d = ops.x - m.x, e0 = ops.y0 - m.y0, e3 = ops.y3 - m.y3;
  if (d >= 63) wait_n(63);
  else if (e0 && v == 0) wait_n(d + e0);
  else if (e3 && v == 3) wait_n(d + e3);
  else wait_n(d);
}
#define WAITM(m) wait_mark(ops, m, v)

__device__ __forceinline__ f32x2 add2(f32x4 e, f32x4 o) { float a = e[0] + o[2]; PIN(a); return f32x2{a, e[1] + o[3]}; }
__device__ __forceinline__ void ldw(u32x4& d, rsrc_t r, int vo, int so) { asm volatile("buffer_load_dwordx4 %0, %1, %2, %3 offen nt" : "=v"(d) : "v"(vo), "s"(r), "s"(so) : "memory"); }
__device__ __forceinline__ void stouch(u64& sd, const char* p, int off) { asm volatile("s_load_dwordx2 %0, %1, %2" : "+s"(sd) : "s"(p), "n"(off) : "memory"); }
constexpr int usched[] = U_SCHED, wsched[] = W_SCHED, tsched[] = T_SCHED, hsched[] = H_SCHED, vsched[] = V_SCHED;
constexpr int prefix(const int* a, int n) { int s = 0; for (int i = 0; i < n; ++i) s += a[i]; return s; }
static_assert(prefix(wsched, NBAR + 1) == 23 && prefix(tsched, NBAR + 1) + prefix(usched, NBAR + 1) == 128 && prefix(hsched, NBAR + 1) == 30 && prefix(vsched, NBAR + 1) == 8, "");
constexpr int useq(int i) { for (int s = -1; s < NBAR; ++s) if (usched[s + 1] && i-- == 0) return s; return NBAR; }
constexpr int uidx(int sl) { int n = 0; for (int s = -1; s < sl; ++s) n += usched[s + 1] > 0; return n; }
static_assert(uidx(NBAR) <= 4, "");
template <class Fn, int... I>
__device__ __forceinline__ void static_for(Fn& fn, std::integer_sequence<int, I...>) { (fn(std::integral_constant<int, I>{}), ...); }

__device__ __forceinline__ void service(int v, int lane, int b, char* lds, karg_t ka, u64 px, u64 pre, u64 pim, u64 pc, int tp1, int tq1 STAMP_ARG) {
  STAMP_DECL;
  volatile LAS unsigned* misc = (volatile LAS unsigned*)(lds + L_MISC);
  const unsigned l0 = (unsigned)(uintptr_t)(LAS char*)lds;
  Cnt ops = {0, 0, 0}, m1[16], m2[14], m3[4], mam, mxe, mw[23], mgg, mp2;
  int va0 = ((lane >> 5) * 8 + (lane >> 3 & 3)) * (D * 2) + ((lane ^ lane >> 3) & 7) * 16;
  int va1 = va0 + 16 * (D * 2) - 1024, va2 = va0 + 4 * (D * 2) - 2048, va3 = va0 + 20 * (D * 2) - 3072;
  const unsigned utab = v >> 1 ? 0x8541u : 0x7230u;
  int k0 = KROT(tp1) * 128;
  unsigned g1o = ((v < 2 ? tp1 : tq1 & 7) * 128 + (v & 1) * 64) * (D * 2), ld1 = l0 + (v & 1) * 8192;
  asm volatile("s_waitcnt lgkmcnt(0)" : "+s"(px), "+s"(pre), "+s"(pim), "+s"(g1o), "+s"(k0), "+s"(ld1), "+v"(va0), "+v"(va1), "+v"(va2), "+v"(va3));
  const rsrc_t r1 = mk_rsrc((const char*)(v < 2 ? px : tq1 & 8 ? pim : pre) + g1o, 64 * D * 2);
#define HALF1(n) do { DMA4("", r1, va0, va1, va2, va3, ((((n) >> 1) * 128 + k0) & 2047) + ((n) & 1) * 32 * D * 2, \
    ld1 + ((utab >> ((((n) >> 1) & 3) * 4)) & 15) * UB + ((n) & 1) * 4096); ops += 4; m1[(n) >> 1] = ops; } while (0)
#pragma unroll
  for (int n = 0; n < 2 + hsched[0]; ++n) HALF1(n);
  STAMP(1);
  asm volatile("" : "+s"(b), "+s"(v), "+s"(tp1), "+s"(tq1), "+v"(lane));
  __builtin_amdgcn_sched_barrier(0);
  __builtin_assume(lane >= 0 && lane < 64 && b >= 0 && b < 256 && v >= 0 && v < 4 && tp1 >= 0 && tp1 < 16 && tq1 >= 0 && tq1 < 16);
  const int rsub = lane >> 3, ch = ((lane & 7) ^ rsub) * 16;
  const int lc = lane & 15, lq = lane >> 4;
  const int z = tq1 >> 3, te = tq1 & 7, h = tp1 >> 3;
  u64 ktab;
  {
    const unsigned ta = (tp1 & 2) ? ((tp1 & 1) ? 0x2107653u : 0x3107652u) : ((tp1 & 1) ? 0x3207651u : 0x3217650u);
    ktab = ((u64)((tp1 & 1) ? 0x4cu : 0xc4u) << 28 | ta) ^ (0x111111111ull * (unsigned)(tp1 & 12));
  }
  const int kfl = tp1 & 1;
#define KTILE(m) ((int)(ktab >> ((m) * 4) & 15) * 2)
#define KSTEP(n) (KTILE((n) >> 1) + ((n) < 2 ? (n) & 1 : ((n) & 1) ^ kfl))
  int vq0 = rsub * (S * 2) + ch, vq1 = vq0 + 8 * S * 2 - 1024, vq2 = vq0 + 16 * S * 2 - 2048, vq3 = vq0 + 24 * S * 2 - 3072;
  int vp = lane * 16;
  u64 sd = 0;
  const int qa = (((v >> 1) ^ h) * 1024 + (v & 1) * 64) * (S * 2), qb = (h * 1024 + v * 32) * (S * 2);
#define QBIG(n) do { const int k_ = qa + KSTEP(n) * 128; const unsigned d_ = l0 + ((n) % 3) * SLOT + v * 8192; \
    DMA4("", rq, vq0, vq1, vq2, vq3, k_, d_); DMA4("", rq, vq0, vq1, vq2, vq3, k_ + 32 * S * 2, d_ + 4096); ops += 8; } while (0)
#define PIMG(n, u, q) DMA4(" sc1", rp, vp, vp, vp, vp, KSTEP(n) * UB + (q) * 4096, l0 + (u) * UB + (q) * 4096)
#define QS(j, u) do { DMA4("", rq, vq0, vq1, vq2, vq3, qb + KSTEP(14 + (j)) * 128, l0 + (u) * UB + v * 4096); ops += 4; m3[j] = ops; } while (0)
#define PS(j, u) do { PIMG(14 + (j), u, v); ops += 4; m3[j] = ops; } while (0)
  const int bf = b ^ 3, rgm = bf * 2341 >> 16, gm = rgm & 1, rg = rgm >> 1, cg = bf - rgm * 28, fg = bf >> 3, eg = bf & 7;
  int vgu = lq * 16 * F * 2 + lc * 16;
  f32x4 ve = {0.f, 0.f, 0.f, 0.f}, vo = ve;
  u32x4 w[16], wd[8], tb, am, fin[4];
  unsigned tag, tch = 0, xe, key, sg, fl, pl;
  int idx;
  unsigned mask = 0;
  unsigned hp, xf;
  u32x4 da = {0u, 0u, 0u, 0u}, db = da;
  u32x2 gg[4];
#define DG1(a, t, k, d) a[d] = (unsigned)__builtin_amdgcn_update_dpp((int)a[d], (int)t, (d) == (k) ? 0xe4 : 0x120 + (4 * ((d) - (k)) & 15), 0xf, 1 << (d), false)
#define DIAG(a, t, k) do { DG1(a, t, k, 0); DG1(a, t, k, 1); DG1(a, t, k, 2); DG1(a, t, k, 3); } while (0)
  int fo = WS_FLAG1 + tq1 * 2048 + (int)(ktab >> (4 + (lane & 28)) & 15) * 128 + (lane & 3) * 4;
#define NEED(need) do { mask |= (unsigned)ALLEQ(fl, tag); if (__builtin_expect((mask & (need)) != (need), 0)) \
    _Pragma("unroll 1") for (int spins = 0; (mask & (need)) != (need) && spins < SPIN_CAP; ++spins) { \
      asm volatile("buffer_load_dword %0, %1, %2, 0 offen sc1\n\ts_waitcnt vmcnt(0)" : "+v"(fl) : "v"(fo), "s"(rws) : "memory"); \
      mask |= (unsigned)ALLEQ(fl, tag); } } while (0)
#define GGLD() asm volatile("buffer_load_dwordx2 %0, %4, %5, 0 offen sc1\n\tbuffer_load_dwordx2 %1, %4, %5, %6 offen sc1\n\t" \
                     "buffer_load_dwordx2 %2, %4, %5, %7 offen sc1\n\tbuffer_load_dwordx2 %3, %4, %5, %8 offen sc1" \
                     : "+&v"(gg[0]), "+&v"(gg[1]), "+&v"(gg[2]), "+&v"(gg[3]) : "v"(guo), "s"(rws), "s"(F * 8), "s"(4 * F * 8), "s"(5 * F * 8) : "memory")
#define FINLD() asm volatile("buffer_load_dwordx4 %0, %4, %5, 0 offen sc1\n\tbuffer_load_dwordx4 %1, %4, %5, %6 offen sc1\n\t" \
                     "buffer_load_dwordx4 %2, %4, %5, %7 offen sc1\n\tbuffer_load_dwordx4 %3, %4, %5, %8 offen sc1" \
                     : "+&v"(fin[0]), "+&v"(fin[1]), "+&v"(fin[2]), "+&v"(fin[3]) : "v"(fgo), "s"(rws), "s"(8 * D * 8), "s"(16 * D * 8), "s"(24 * D * 8) : "memory")
  auto final_sum = [&]() __attribute__((always_inline)) {
      float y0 = (__uint_as_float(fin[0][0]) + __uint_as_float(fin[1][0])) + (__uint_as_float(fin[2][0]) + __uint_as_float(fin[3][0]));
      float y1 = (__uint_as_float(fin[0][2]) + __uint_as_float(fin[1][2])) + (__uint_as_float(fin[2][2]) + __uint_as_float(fin[3][2]));
      DPP_ADD(y0, 0x128, 0xf, false); DPP_ADD(y1, 0x128, 0xf, false);
      y0 += __shfl_xor(y0, 16); y1 += __shfl_xor(y1, 16);
      y0 += __shfl_xor(y0, 32); y1 += __shfl_xor(y1, 32);
      const unsigned srt = key >> 16;
      const float r = lo2f((srt & 0x8000u) ? (srt & 0x7fffu) : (~srt & 0xffffu));
      const float rsel = rbf(1.0f / (1.0f + __expf(-r)));
      *(LAS f32x2*)(lds + L_UPD + v * 64 + (lane & 7) * 8) = f32x2{rbf(rbf(y0) * rsel), rbf(rbf(y1) * rsel)};
      WAIT_LGKM0();
      if (lane == 0) misc[16 + v] = 1u;
      STAMP(10);
  };
  int tbo = WS_STAT + tp1 * 1024 + vp, amo = WS_AMAX + v * 1024 + vp;
  int pt0 = WS_XCH + b * XB + v * XW + 13824, pt1 = WS_FLAG1 + (tq1 * 16 + tp1) * 128;
  asm volatile("" : "+s"(pt0), "+s"(pt1));
  int pto = lane < 2 ? pt0 + lane * 128 : pt1;
  int cmo = L_CMB + v * 512 + (lc * 4 + lq) * 8, xeo = ((rg & 3) * 256 + v * 64 + lq * 16 + (lc >> 1)) * 2, l8 = lane * 8;
  int fo1 = WS_FLAG1 + tq1 * 2048 + (KSTEP(2) >> 1) * 128 + (lane & 3) * 4, amp = lane == 4 ? WS_AMAX + b * 16 : lane < 4 ? WS_STAT + (tp1 * 128 + tq1 * 8) * 8 + vp : OOB;
  PIN4(tbo, amo, fo, fo1); PIN4(pto, cmo, xeo, l8); PIN4(vq0, vq1, vq2, vq3); PIN4(vgu, vp, da, db); PIN(amp);
  int guo, fgo, puo, pdo, co, vdn, vdl, uo[4];
  unsigned hmask;
  auto setup2 = [&]() __attribute__((always_inline)) {
    const int l = lane_now(), c = l & 15, q = l >> 4;
    __builtin_assume(l >= 0 && l < 64);
    guo = WS_GU + ((c & 1) * 2 * F + fg * 112 + v * 28 + (c < 12 ? (c & 12) * 2 + q * 2 + (c >> 1 & 1) : 24 + q)) * 8;
    fgo = WS_PART + ((l >> 3) * D + te * 128 + z * 64 + v * 16 + (l & 7) * 2) * 8;
    puo = (b < 224 ? WS_GU + bf * 1024 : OOB - 1024) + l * 16; pdo = WS_PART + bf * 1024 + l * 16;
    co = WS_XCH + (b ^ (0x462 >> l * 4 & 15)) * XB + v * XW + (l == 2 ? 8192 + 5124 : 13824 + l * 128);
    PIN(co);
    co = l < 3 ? co : OOB;
    vdn = q * 2 * D * 2 + c * 16; vdl = (24 + q) * D * 2 + c * 16;
    hmask = l & 1 ? 0xffff0000u : 0xffffu;
#define UOFF(i) if constexpr (useq(i) < NBAR) { constexpr int sl = useq(i), cnt = usched[sl < NBAR ? sl + 1 : 0], n0 = prefix(tsched, NBAR + 1) + prefix(usched, sl + 1); \
      static_assert(sl > 18, ""); const int n = n0 + min(l, cnt - 1), m = n < 112 ? n >> 4 : (n >> 3) - 7, r = n < 112 ? n & 15 : n & 7; \
      const int ra = (te >> 2) * 1024 + ((te & 3) * 4 + v) * 8, rb = h * 1024 + (te * 4 + v) * 4; \
      uo[i] = ((n < 112 ? ra : rb) + (r >> 1)) * (S * 2) + ((int)(ktab >> (m * 4) & 15) * 2 + (r & 1)) * 128; PIN(uo[i]); }
    UOFF(0) UOFF(1) UOFF(2) UOFF(3)
    PIN4(guo, fgo, puo, pdo); PIN4(co, vdn, vdl, hmask);
  };
  if (v == 3 && lane < 8) misc[12 + lane] = 0u;
  WAITM(m1[0]);
  BAR();
#pragma unroll
  for (int n = 2 + hsched[0]; n < 2 + hsched[0] + PRE_H; ++n) HALF1(n);
  asm volatile("" : "+s"(ka));
  __builtin_amdgcn_sched_barrier(0);
  const char* ws = (const char*)ka[9];
  const char* gq = (const char*)ka[1 + z] + (size_t)(tp1 & 7) * 128 * S * 2;
  const rsrc_t rws = mk_rsrc(ws, WS_END), rx = mk_rsrc((const void*)px, S * D * 2);
  const rsrc_t rq = mk_rsrc(gq, 1152 * S * 2), rp = mk_rsrc(ws + WS_ABT + (size_t)tq1 * ABT_ST, 128 * S * 2);
  const rsrc_t rgu = mk_rsrc((const u16*)ka[6 + gm] + (size_t)((rg & 3) * 256 + v * 64) * F + cg * 128, b < 224 ? 64 * F * 2 : 0);
  const rsrc_t rd = mk_rsrc((const u16*)ka[8] + (size_t)(fg * 112 + v * 28) * D + eg * 128, 28 * D * 2);
  const char* ga_ = gq + (size_t)((te >> 2) * 1024 + ((te & 3) * 4 + v) * 8) * (S * 2);
  const char* gb_ = gq + (size_t)(h * 1024 + (te * 4 + v) * 4) * (S * 2);
  asm volatile("" :: "s"(rws), "s"(rq), "s"(rp), "s"(rgu), "s"(rd), "s"(ga_));
  WAIT_LGKM0();
#define UNTIL(pin, ok, reload) do { pin; if (__builtin_expect(!(ok), 0)) { _Pragma("unroll 1") for (int spins = 0; spins < SPIN_CAP; ++spins) { \
      __builtin_amdgcn_s_sleep(2); reload; wait_n(0); pin; if (ok) break; } } } while (0)
  auto work = [&](auto slc) __attribute__((always_inline)) {
    constexpr int sl = decltype(slc)::value - 1;
    if (sl == AT_AT) {
      WAITM(mam);
      UNTIL(asm volatile("" : "+v"(tb), "+v"(am)), (ALLEQ(tb[1], tag) & ALLEQ(tb[3], tag) & ALLEQ(am[1], tag) & ALLEQ(am[3], tag)) == ~0ull,
            asm volatile("buffer_load_dwordx4 %0, %2, %4, 0 offen sc1\n\tbuffer_load_dwordx4 %1, %3, %4, 0 offen sc1" : "+v"(tb), "+v"(am) : "v"(tbo), "v"(amo), "s"(rws) : "memory"));
      if (v == 0) *(LAS u32x2*)(lds + L_MB + l8) = u32x2{tb[0] << 16 ^ 0x80000000u, tb[2] << 16 ^ 0x80000000u};
      if (v == 1) *(LAS f32x2*)(lds + L_MB + 512 + l8) = f32x2{__builtin_amdgcn_rcpf(hi2f(tb[0])), __builtin_amdgcn_rcpf(hi2f(tb[2]))};
      const unsigned mk = wave_maxu(am[0]);
      if (am[0] == mk) *(LAS u32x2*)(lds + L_MISC + 16 + v * 8) = u32x2{mk, am[2]};
    }
    if (sl == XN_AT) {
      WAITM(mxe);
      PIN(xe); PIN(xf);
      const unsigned n_ = ln_pair(xe | xf << 16, -lo2f(sg), __builtin_amdgcn_rcpf(hi2f(sg)));
      xe = lane & 1 ? n_ << 16 : n_ & 0xffffu; xf = lane & 1 ? n_ & 0xffff0000u : n_ >> 16;
      PIN(xe); PIN(xf);
    }
    constexpr int v0 = prefix(vsched, sl + 1), vn = vsched[sl + 1];
    if constexpr (vn > 0) {
      static_assert(prefix(wsched, sl + 1) >= 2 * (v0 + vn) && sl >= XN_AT && sl < CGU_AT, "");
      WAITM(mw[2 * (v0 + vn) - 1]);
#define VPAIR(k) if ((k) >= v0 && (k) < v0 + vn) { PIN(w[2 * (k)]); PIN(w[2 * (k) + 1]); \
        if ((k) & 1) DIAG(db, ((k) < 4 ? xe : xf), (k) & 3); else DIAG(da, ((k) < 4 ? xe : xf), (k) & 3); \
        ve = MFMA(BF8((k) & 1 ? db : da), BF8(w[2 * (k)]), ve); vo = MFMA(BF8((k) & 1 ? db : da), BF8(w[2 * (k) + 1]), vo); }
      VPAIR(0) VPAIR(1) VPAIR(2) VPAIR(3) VPAIR(4) VPAIR(5) VPAIR(6) VPAIR(7)
      if (v0 + vn == 8) *(LAS f32x2*)(lds + cmo) = add2(ve, vo);
      else asm volatile("" : "+v"(ve), "+v"(vo));
    }
    if (sl == GD_AT) {
      WAITM(mgg);
      UNTIL(PIN4(gg[0], gg[1], gg[2], gg[3]), (ALLEQ(gg[0][1], tag) & ALLEQ(gg[1][1], tag) & ALLEQ(gg[2][1], tag) & ALLEQ(gg[3][1], tag)) == ~0ull, GGLD());
      float g_ = __uint_as_float(gg[0][0]) + __uint_as_float(gg[2][0]);
      PIN(g_);
      float u_ = __uint_as_float(gg[1][0]) + __uint_as_float(gg[3][0]);
      ADD2_DPP(g_, u_, "quad_perm:[1,0,3,2]");
      const unsigned gu = pack2(g_, u_);
      const float h_ = rbf(silu_fast(lo2f(gu))) * hi2f(gu);
      hp = pack2(h_, h_) & hmask;
      PIN(hp);
    }
    if (sl == GE_AT) {
      static_assert(prefix(wsched, GE_AT + 1) == 23 && GE_AT >= GD_AT, "");
      WAITM(mw[22]);
      PIN4(wd[0], wd[1], wd[2], wd[3]); PIN4(wd[3], wd[4], wd[5], wd[6]);
      f32x4 ae = {0.f, 0.f, 0.f, 0.f}, ao = ae;
#define DPAIR(i) { if ((i) & 1) DIAG(db, hp, i); else DIAG(da, hp, i); \
        ae = MFMA(BF8((i) & 1 ? db : da), BF8(wd[2 * (i)]), ae); if ((i) < 3) ao = MFMA(BF8((i) & 1 ? db : da), BF8(wd[2 * (i) + 1]), ao); }
      DPAIR(0) DPAIR(1) DPAIR(2) DPAIR(3)
      PIN(ae); PIN(ao);
      *(LAS f32x2*)(lds + cmo) = add2(ae, ao);
    }
  };
  auto slot = [&](auto slc) __attribute__((always_inline)) {
    constexpr int sl = decltype(slc)::value - 1, nw = prefix(wsched, sl + 1), nt = prefix(tsched, sl + 1);
    if constexpr (sl > 0) {
      if constexpr (sl < 16) WAITM(m1[sl]);
      else if constexpr (sl == 19) { if (v == 0) WAITM(mp2); else WAITM(m2[2]); }
      else if constexpr (sl >= 17 && sl < 31) WAITM(m2[sl - 17]);
      else if constexpr (sl >= 31) WAITM(m3[sl == 31 ? 0 : sl - 30]);
      if (sl == 19 && v == 0) { PIN(fl); NEED(0xf0u); }
      if constexpr (sl >= 21 && sl <= NEED_TO && (sl & 1)) { if (v == 0) { PIN(fl); NEED(sl == NEED_TO ? ~0u : 0xfu << 2 * (sl - 17)); } }
      BAR();
    }
    u32x4 pub;
    constexpr bool cgu = sl == CGU_AT, cdn = sl == GE_AT + 1;
    if (sl == SC_AT) {
      if (v == 3) {
        tag = rfl(misc[2]);
        const u32x4 k0 = *(const LAS u32x4*)(lds + L_MISC + 80), k1 = *(const LAS u32x4*)(lds + L_MISC + 96);
        const unsigned k_ = max(max(max(k0[0], k0[1]), max(k0[2], k0[3])), max(max(k1[0], k1[1]), max(k1[2], k1[3])));
        const u32x2 sp = *(const LAS u32x2*)(lds + L_MISC + 112 + (l8 & 24));
        const unsigned sk = misc[28 + (~k_ & 7)];
        pub = lane < 4 ? u32x4{sp[0], tag, sp[1], tag} : u32x4{k_, tag, sk, tag};
        PIN(pub);
      }
    }
    if ((cgu || cdn) && v == 3) {
      const LAS f32x2* c_ = (const LAS f32x2*)(lds + L_CMB + l8);
      const f32x2 c0 = c_[0], c1 = c_[64], c2 = c_[128], c3 = c_[192];
      float s0 = c0[0] + c1[0], s1 = c2[0] + c3[0]; PIN(s0); PIN(s1); s0 += s1; PIN(s0);
      float s2 = c0[1] + c1[1], s3 = c2[1] + c3[1]; PIN(s2); PIN(s3);
      pub = u32x4{__float_as_uint(s0), tag, __float_as_uint(s2 + s3), tag};
      PIN(pub);
    }
    if (sl >= 19 && sl < NEED_TO && (sl & 1)) { if (v == 0) asm volatile("buffer_load_dword %0, %1, %2, 0 offen sc1" : "+v"(fl) : "v"(fo), "s"(rws) : "memory"); ++ops.y0; }
    constexpr int h0 = 2 + prefix(hsched, sl + 1), hn = hsched[sl + 1];
    static_assert(hn == 0 || h0 + hn <= 2 * (sl + 4), "image not free yet");
#pragma unroll
    for (int n = h0 + (sl == 0 ? PRE_H : 0); n < h0 + hn && sl >= 0; ++n) HALF1(n);
    if (sl == Q0_AT) { QBIG(0); m2[0] = ops; }
    if (sl == Q1_AT) { QBIG(1); m2[1] = ops; }
    if (sl == Q2_AT) { QBIG(2); m2[2] = ops; }
    if (sl == 18) {
      mp2 = ops;
      if (v != 0) { QBIG(3); ops += -8; }
      if (v == 0) {
        STAMP(2);
        unsigned f1, f2;
        int cap = SPIN_CAP;
        asm volatile("buffer_load_dword %0, %4, %5, 0 offen sc1\n\ts_sleep 3\n\tbuffer_load_dword %1, %4, %5, 0 offen sc1\n\ts_sleep 3\n"
                     "1:\n\tbuffer_load_dword %2, %4, %5, 0 offen sc1\n\ts_waitcnt vmcnt(2)\n\tv_cmp_ne_u32 vcc, %6, %0\n\ts_cbranch_vccz 2f\n\t"
                     "buffer_load_dword %0, %4, %5, 0 offen sc1\n\ts_waitcnt vmcnt(2)\n\tv_cmp_ne_u32 vcc, %6, %1\n\ts_cbranch_vccz 2f\n\t"
                     "buffer_load_dword %1, %4, %5, 0 offen sc1\n\ts_waitcnt vmcnt(2)\n\tv_cmp_ne_u32 vcc, %6, %2\n\ts_cbranch_vccz 2f\n\t"
                     "s_sub_u32 %3, %3, 1\n\ts_cmp_lg_u32 %3, 0\n\ts_cbranch_scc1 1b\n2:"
                     : "=&v"(f1), "=&v"(f2), "=&v"(fl), "+s"(cap) : "v"(fo1), "s"(rws), "s"(tag) : "memory", "vcc", "scc");
        STAMP(3);
        PIMG(2, 8, 0); PIMG(2, 8, 1); PIMG(2, 8, 2); PIMG(2, 8, 3);
        asm volatile("buffer_load_dword %0, %3, %4, 0 offen sc1" : "+v"(fl), "+v"(f1), "+v"(f2) : "v"(fo), "s"(rws) : "memory");
        QBIG(3); ops += -8;
        PIMG(3, 2, 0); PIMG(3, 2, 1); PIMG(3, 2, 2); PIMG(3, 2, 3);
        asm volatile("s_waitcnt vmcnt(41)" : "+v"(f1), "+v"(f2) :: "memory");
      }
      ops += 8; ops.y0 += 16; m2[3] = ops;
    }
    if constexpr (sl >= 19 && sl < 29) { QBIG(sl - 15); PIMG(sl - 15, ((sl - 15) % 3) * 3 + 2, v); ops += 4; m2[sl - 15] = ops; }
    if (sl == 29) { QS(0, 6); PS(0, 8); PS(1, 7); }
    if (sl == 30) { QS(1, 0); QS(2, 1); PS(2, 2); }
    if (sl == 31) { QS(3, 3); PS(3, 5); }
    if (sl == SC_AT) { if (v == 3) ST4(pub, amp); else tag = rfl(misc[2]); ++ops.y3; }
    if (cgu && v == 3) ST4(pub, puo);
    if (cdn && v == 3) ST4(pub, pdo);
    if (cgu || cdn) ++ops.y3;
    if (sl == PT_AT) {
      TOUCH(" sc1", rws, pto);
    }
    if (sl == AM_AT) { LD4Z(" sc1", tb, rws, tbo); LD4Z(" sc1", am, rws, amo); mam = ops; }
    if (sl == GG_AT) { GGLD(); ops += 4; mgg = ops; }
    if (sl == XE_AT) {
      const u32x4 k0 = *(const LAS u32x4*)(lds + L_MISC + 16), k1 = *(const LAS u32x4*)(lds + L_MISC + 32);
      key = k0[0]; sg = k0[1];
      if (k0[2] > key) { key = k0[2]; sg = k0[3]; }
      if (k1[0] > key) { key = k1[0]; sg = k1[1]; }
      if (k1[2] > key) { key = k1[2]; sg = k1[3]; }
      idx = min(0xffff - (int)(rfl(key) & 0xffffu), S - 1);
      asm volatile("s_nop 4\n\tbuffer_load_ushort %0, %2, %3, %4 offen\n\tbuffer_load_ushort %1, %2, %3, %4 offen offset:16" : "=&v"(xe), "=&v"(xf)
                   : "v"(xeo), "s"(rx), "s"(idx * D * 2) : "memory");
      ops += 2;
      mxe = ops;
    }
    if (sl == AT_AT + 1) { if (v == 3) ST1(tag, b == 0 && lane == 0 ? (int)WS_EPOCH : OOB); ++ops.y3; }
    if constexpr (usched[sl + 1] > 0) { constexpr int ui = uidx(sl); const int o_ = uo[ui]; TOUCH("", rq, o_); }
    constexpr int tn = tsched[sl + 1], wn = wsched[sl + 1];
#pragma unroll
    for (int k = nw; k < nw + wn; ++k) {
      if (k < 16) ldw(w[k & 15], rgu, vgu, k * F * 2);
      else ldw(wd[(k - 16) & 7], rd, k < 22 ? vdn : vdl, k < 22 ? (((k - 16) >> 1) * 8 + (k & 1)) * D * 2 : 0);
      ++ops; mw[k] = ops;
    }
    work(slc);
    if (sl == SET2_AT) { static_assert(prefix(wsched, 20) <= 16 && CGU_AT > 18, ""); setup2(); }
    if (sl == NBAR - 1) {
      unsigned got = 0;
      bool own = (idx >> 7) == tp1;
#pragma unroll 1
      for (int spins = 0; spins < SPIN_CAP && (got != 7 || own); ++spins) {
        if (own) FINLD();
        asm volatile("buffer_load_dword %0, %1, %2, 0 offen sc1\n\ts_waitcnt vmcnt(0)" : "=&v"(pl) : "v"(co), "s"(rws) : "memory");
        const unsigned g_ = got | ((unsigned)ALLEQ(pl, tag) & 7u);
        if (g_ != got) { got = g_; misc[12 + v] = got; if (got >> (lane & 3) & 1) co = OOB; }
        if (own) {
          PIN4(fin[0], fin[1], fin[2], fin[3]);
          unsigned bad = 0;
#pragma unroll
          for (int k = 0; k < 4; ++k) bad |= (fin[k][1] ^ tag) | (fin[k][3] ^ tag);
          if (__all(bad == 0)) { final_sum(); own = false; }
        }
        __builtin_amdgcn_s_sleep(2);
      }
      STAMP(11);
    }
    constexpr bool wrote = sl == AT_AT || sl == GE_AT || (vsched[sl + 1] > 0 && prefix(vsched, sl + 2) == 8);
    if (wrote) WAIT_LGKM0();
    if (sl == CT2_AT || sl == CT3_AT) {
      if (v == 3) asm volatile("global_load_dword %0, %1, %2" : "+v"(tch) : "v"(min((b >> 3 & 7) + lane * 8, CODE_LINES - 1) * 128), "s"(pc) : "memory");
      ++ops.y3;
    }
#pragma unroll
    for (int n = nt; n < nt + tn; ++n) {
      const int m = n < 112 ? n >> 4 : (n >> 3) - 7, r = n < 112 ? n & 15 : n & 7;
      stouch(sd, (n < 112 ? ga_ : gb_) + KTILE(m) * 128, (r >> 1) * S * 2 + (r & 1) * 128 + 60);
    }
  };
  static_for(slot, std::make_integer_sequence<int, NBAR + 1>{});
  asm volatile("s_waitcnt vmcnt(0) lgkmcnt(0)" : "+s"(sd), "+v"(tch) :: "memory");
  STAMP_OUT();
}

#define LDF(off) (*reinterpret_cast<const bf16x8*>(lds + (off)))
#define SB() __builtin_amdgcn_sched_barrier(0)
__device__ __forceinline__ void mm(f32x4& c, const bf16x8& a, const bf16x8& b, bool zero) {
  if (zero) asm volatile("v_mfma_f32_16x16x32_bf16 %0, %1, %2, 0" : "=&v"(c) : "v"(a), "v"(b));
  else asm volatile("v_mfma_f32_16x16x32_bf16 %0, %1, %2, %0" : "+v"(c) : "v"(a), "v"(b));
}
__device__ __forceinline__ void mm2(f32x4& c, const bf16x8& a, const bf16x8& b, const bf16x8& a2, const bf16x8& b2, bool zero) {
  if (zero) asm volatile("v_mfma_f32_16x16x32_bf16 %0, %1, %2, 0\n\tv_mfma_f32_16x16x32_bf16 %0, %3, %4, %0" : "=&v"(c) : "v"(a), "v"(b), "v"(a2), "v"(b2));
  else asm volatile("v_mfma_f32_16x16x32_bf16 %0, %1, %2, %0\n\tv_mfma_f32_16x16x32_bf16 %0, %3, %4, %0" : "+v"(c) : "v"(a), "v"(b), "v"(a2), "v"(b2));
}
#define MWAIT() do { asm volatile("s_nop 7\n\ts_nop 7"); SB(); } while (0)
#define BLOCK(a, G0, Q0, ZERO, X) do { _Pragma("unroll") for (int n_ = 0; n_ < 16; ++n_) { \
    const int i_ = n_ >> 2, g_ = (i_ ^ n_ >> 1) & 1, h_ = (n_ ^ n_ >> 1) & 1; \
    if (n_ < 6) mm(a[i_][(G0) + g_], p[h_][i_], q[(Q0) + 2 * g_ + h_], (ZERO) && !(n_ & 1)); \
    else if (!(n_ & 1)) mm2(a[i_][(G0) + g_], p[h_][i_], q[(Q0) + 2 * g_ + h_], p[1 - h_][i_], q[(Q0) + 2 * g_ + 1 - h_], ZERO); \
    SB(); X(n_); SB(); } } while (0)
#define LDQ(n, N0, Q0, a0, a1) if ((n) >= (N0) && (n) < (N0) + 4) q[(Q0) + (n) - (N0)] = LDF((((n) - (N0)) & 1 ? (a1) : (a0)) + (((n) - (N0)) >> 1) * 2048)
#define LDP(n, N0, i, a0, a1) if ((n) == (N0) || (n) == (N0) + 1) p[(n) - (N0)][i] = LDF(((n) == (N0) ? (a0) : (a1)))

__device__ __forceinline__ void consumer(int wid, int lane, int b, char* lds, karg_t ka, unsigned tch, int tp1, int tq1 STAMP_ARG) {
  STAMP_DECL;
  int g_cb = L_DBG;
  const rsrc_t rws = mk_rsrc((const void*)ka[9], WS_END), rx = mk_rsrc((const void*)ka[0], S * D * 2), rwr = mk_rsrc((const void*)ka[5], D * 2);
  u64 pout = ka[10];
  asm volatile("" : "+s"(pout));
  const int z = tq1 >> 3, te = tq1 & 7;
  const int wp = wid >> 1, wq = wid & 1, fr = lane & 15, fq = lane >> 4;
  const int c0 = (fq ^ (fr & 7)) * 16, c1 = ((fq + 4) ^ (fr & 7)) * 16;
  const int q0 = (wq * 64 + fr) * 128 + c0, q1 = (wq * 64 + fr) * 128 + c1;
  volatile LAS unsigned* misc = (volatile LAS unsigned*)(lds + L_MISC);
  f32x4 acc[4][4];
#pragma unroll
  for (int i = 0; i < 4; ++i)
#pragma unroll
    for (int j = 0; j < 4; ++j) acc[i][j] = f32x4{0.f, 0.f, 0.f, 0.f};
  bf16x8 p[2][4], q[8];
  unsigned tag = 0;
  u32x4 xv[4];
#define XO(l) (((tp1 * 128 + wq * 64 + ((l) & 15)) * D + te * 128 + z * 64 + wp * 32 + ((l) >> 4) * 8) * 2)
  {
    const int xt = (2 * te + z - KROT(tp1)) & 15;
    const int xrow = wq * 64 + 16 * ((fr >> 2) & 1) + 4 * (fr >> 3) + (fr & 3);
    const int xr = xrow * 128 + (((wp * 4 + fq) ^ (xrow & 7)) * 16);
#define XV(u) do { _Pragma("unroll") for (int k = 0; k < 4; ++k) \
      xv[k] = *reinterpret_cast<const u32x4*>(lds + (u) * UB + xr + (k >> 1) * 4096 + (k & 1) * 1024); } while (0)
    const int ca = wp ? c1 : c0, cb = wp ? c0 : c1;
    int p0 = (wp * 64 + fr) * 128 + ca, p1 = (wp * 64 + fr) * 128 + cb, w0 = (wq * 64 + fr) * 128 + ca, w1 = (wq * 64 + fr) * 128 + cb;
    f32x4 acs[4];
    bf16x8 ones = BF8((u32x4{0x3f803f80u, 0x3f803f80u, 0x3f803f80u, 0x3f803f80u}));
    u32x4 st[4], wv[4];
    unsigned epoch;
    float sw;
    const int row0 = tp1 * 128 + tq1 * 8 + wid * 2;
    char* sc = lds + L_ST + wid * 2048;
    int o1 = WS_AMAX + b * 16, o2 = WS_STAT + row0 * 8;
    asm volatile("" : "+s"(o1), "+s"(o2));
    int eo = lane == 0 ? WS_EPOCH : lane == 1 ? o1 : o2, wo = ((fr & 7) + fq * 8) * 16, vo = (row0 + (fr >> 3)) * (D * 2) + wo;
#pragma unroll
    for (int i = 0; i < 4; ++i) wv[i] = __builtin_bit_cast(u32x4, ones);
    PIN4(wv[0], wv[1], wv[2], wv[3]);
    asm volatile("" : "+v"(eo), "+v"(wo), "+v"(vo) : "s"(rws), "s"(rx), "s"(rwr));
    {
      asm volatile("buffer_load_dword %0, %5, %6, 0 offen sc1\n\t"
                   "s_mov_b64 exec, %8\n\t"
                   "buffer_load_dwordx4 %1, %9, %7, 0 offen\n\tbuffer_load_dwordx4 %2, %9, %7, 0 offen offset:512\n\t"
                   "buffer_load_dwordx4 %3, %9, %7, 0 offen offset:1024\n\tbuffer_load_dwordx4 %4, %9, %7, 0 offen offset:1536\n\t"
                   "s_mov_b64 exec, -1"
                   : "=&v"(epoch), "+&v"(wv[0]), "+&v"(wv[1]), "+&v"(wv[2]), "+&v"(wv[3])
                   : "v"(eo), "s"(rws), "s"(rwr), "s"(0x00ff00ff00ff00ffull), "v"(wo) : "memory");
    }
    auto stats0 = [&]() __attribute__((always_inline)) {
      asm volatile("buffer_load_dwordx4 %0, %4, %5, 0 offen\n\tbuffer_load_dwordx4 %1, %4, %5, 0 offen offset:512\n\t"
                   "buffer_load_dwordx4 %2, %4, %5, 0 offen offset:1024\n\tbuffer_load_dwordx4 %3, %4, %5, 0 offen offset:1536"
                   : "=&v"(st[0]), "=&v"(st[1]), "=&v"(st[2]), "=&v"(st[3]) : "v"(vo), "s"(rx) : "memory");
    };
    auto stats1 = [&]() __attribute__((always_inline)) {
      f32x4 d1 = {0.f, 0.f, 0.f, 0.f}, d2 = d1, d4 = d1;
      asm volatile("s_waitcnt vmcnt(4)" : "+v"(epoch), "+v"(tch), "+v"(wv[0]), "+v"(wv[1]), "+v"(wv[2]), "+v"(wv[3]) :: "memory");
      tag = mk_tag(epoch);
#pragma unroll
      for (int i = 0; i < 4; ++i) d4 = MFMA(BF8(wv[i]), BF8(wv[i]), d4);
      const int s4 = __builtin_bit_cast(int, (d4[0] + d4[1]) + (d4[2] + d4[3]));
      sw = __builtin_bit_cast(float, __builtin_amdgcn_readlane(s4, 8)) + __builtin_bit_cast(float, __builtin_amdgcn_readlane(s4, 24));
      asm volatile("s_waitcnt vmcnt(0)" : "+v"(st[0]), "+v"(st[1]), "+v"(st[2]), "+v"(st[3]), "+v"(sw) :: "memory");
      STAMP(7);
#pragma unroll
      for (int i = 0; i < 4; ++i) { d2 = MFMA(BF8(st[i]), BF8(st[i]), d2); d1 = MFMA(BF8(st[i]), BF8(wv[i]), d1); }
      *reinterpret_cast<f32x4*>(sc + 1024 + lane * 16) = d2;
      *reinterpret_cast<f32x4*>(sc + lane * 16) = d1;
      asm volatile("" ::: "memory");
      STAMP(11);
    };
    float sa, sx, sq, mbv, den;
    auto stats2 = [&](int n) __attribute__((always_inline)) {
      const int r = fr >> 3;
      if (n == 0) {
        const char* ad = sc + (fr >> 2) * 256 + (fr & 3) * 4;
        sa = *reinterpret_cast<const float*>(ad + (fr & 7) * 16); sx = *reinterpret_cast<const float*>(ad + 128); sq = *reinterpret_cast<const float*>(ad + 1024 + fr * 16);
      }
      if (n == 6) { DPP_ADD(sa, 0xb1, 0xf, true); DPP_ADD(sx, 0xb1, 0xf, true); DPP_ADD(sq, 0xb1, 0xf, true); }
      if (n == 7) { DPP_ADD(sa, 0x4e, 0xf, true); DPP_ADD(sx, 0x4e, 0xf, true); DPP_ADD(sq, 0x4e, 0xf, true); }
      if (n == 8) { DPP_ADD(sa, 0x141, 0xf, true); DPP_ADD(sx, 0x141, 0xf, true); DPP_ADD(sq, 0x141, 0xf, true); }
      if (n == 9) { sx *= 1.0f / D; sq = fmaxf(fmaf(-sx, sx, sq * (1.0f / D)), 0.f); }
      if (n == 10) sq = rbf(rbf(sq) + 1e-5f);
      if (n == 11) { mbv = rbf(sx); den = rbf(__builtin_amdgcn_sqrtf(sq)); }
      if (n == 13) sa = (sa - mbv * sw) * __builtin_amdgcn_rcpf(den);
      if (n == 14) {
        const unsigned rb = pack2(sa, sa) & 0xffffu;
        unsigned srt = (rb & 0x8000u) ? (~rb & 0xffffu) : (rb | 0x8000u);
        if (sa != sa) srt = 0u;
        LAS unsigned* mk = (LAS unsigned*)(lds + L_MISC) + wid * 2 + r;
        mk[20] = (srt << 16) | (unsigned)(0xffff - row0 - r); mk[28] = pack2(mbv, den);
        *(LAS unsigned*)(lds + L_MISC + 8) = tag;
      }
      if (n >= 6 && n < 14) asm volatile("" : "+v"(sa), "+v"(sx), "+v"(sq));
    };
#pragma unroll
    for (int i = 0; i < 4; ++i) { acs[i] = f32x4{0.f, 0.f, 0.f, 0.f}; PIN4(acc[i][0], acc[i][1], acc[i][2], acc[i][3]); }
    PIN4(acs[0], acs[1], acs[2], acs[3]); PIN(ones);
    int ph0 = p0 + 4 * UB, ph1 = p1 + 4 * UB, wh0 = w0 + 5 * UB, wh1 = w1 + 5 * UB, wl0 = w0 + UB, wl1 = w1 + UB;
    PIN4(ph0, ph1, wh0, wh1); PIN4(wl0, wl1, p0, p1);
    CBAR();
    STAMP(12);
#define XU(i) ((0x7230u >> ((i) & 3) * 4) & 15)
#define WU(i) ((0x8541u >> ((i) & 3) * 4) & 15)
#define XA(h, i) (XU(i) < 4 ? p##h + XU(i) * UB : ph##h + (XU(i) - 4) * UB)
#define WA(h, i) (WU(i) < 5 ? wl##h + (WU(i) - 1) * UB : wh##h + (WU(i) - 5) * UB)
#define RSUM(j) mm(acs[j], ones, q[2 * (j)], false)
#define X1A(n) LDP(n, 0, 3, XA(0, i1) + 6144, XA(1, i1) + 6144); LDQ(n, 2, 4, WA(0, i1) + 4096, WA(1, i1) + 4096); if ((n) == 7) RSUM(0); if ((n) == 11) RSUM(1)
#define X1B(n) LDQ(n, 0, 0, WA(0, i1 + 1), WA(1, i1 + 1)); LDP(n, 4, 0, XA(0, i1 + 1), XA(1, i1 + 1)); LDP(n, 8, 1, XA(0, i1 + 1) + 2048, XA(1, i1 + 1) + 2048); \
      LDP(n, 12, 2, XA(0, i1 + 1) + 4096, XA(1, i1 + 1) + 4096); if ((n) == 7) RSUM(2); if ((n) == 11) RSUM(3)
#define X1S(n) stats2(n); X1A(n)
#define STEP1(t, i) do { constexpr int i1 = i; \
      if ((t) == 0) { stats0(); SB(); } \
      if ((t) == 2) BLOCK(acc, 0, 0, false, X1S); else BLOCK(acc, 0, 0, false, X1A); \
      if (__builtin_expect((t) == xt, 0)) XV(XU(i)); \
      if ((t) == 1) stats1(); \
      WAIT_LGKM0(); \
      CBAR(); \
      BLOCK(acc, 2, 4, false, X1B); } while (0)
#pragma unroll
    for (int n = 0; n < 4; ++n) q[n] = LDF((n & 1 ? WA(1, 0) : WA(0, 0)) + (n >> 1) * 2048);
#pragma unroll
    for (int i = 0; i < 3; ++i) { p[0][i] = LDF(XA(0, 0) + i * 2048); p[1][i] = LDF(XA(1, 0) + i * 2048); }
    SB();
    STEP1(0, 0); STEP1(1, 1); STEP1(2, 2);
#pragma unroll 1
    for (int t = 3; t < 15; t += 4) { STEP1(t, 3); STEP1(t + 1, 0); STEP1(t + 2, 1); STEP1(t + 3, 2); }
    if (xt == 15) XV(7);
    f32x4 mb[2][2], rd[2][2];
    const float sgn = z ? -1.f : 1.f;
#define TABS(n) rd[(n) >> 1 & 1][(n) & 1] = *reinterpret_cast<const f32x4*>(lds + L_MB + 512 + (wp * 64 + ((n) >> 1) * 32 + fq * 8 + ((n) & 1) * 4) * 4); \
      mb[(n) >> 1 & 1][(n) & 1] = *reinterpret_cast<const f32x4*>(lds + L_MB + (wp * 64 + ((n) >> 1) * 32 + fq * 8 + ((n) & 1) * 4) * 4)
    {
      constexpr int i1 = 3;
#define X1L(n) LDQ(n, 0, 4, WA(0, i1) + 4096, WA(1, i1) + 4096); LDP(n, 4, 3, XA(0, i1) + 6144, XA(1, i1) + 6144); if ((n) == 1) RSUM(0); if ((n) == 3) RSUM(1); \
      if ((n) == 7) RSUM(2); if ((n) == 9) RSUM(3); \
      if ((n) == 12) *reinterpret_cast<f32x4*>(lds + L_CS + wid * 256 + fr * 16) = f32x4{acs[0][0], acs[1][0], acs[2][0], acs[3][0]}; \
      if ((n) == 13) PIN4(acs[0], acs[1], acs[2], acs[3]); \
      if ((n) >= 6 && (n) < 10) { TABS((n) - 6); } if ((n) == 14) { rd[0][0] *= sgn; rd[0][1] *= sgn; rd[1][0] *= sgn; rd[1][1] *= sgn; PIN4(rd[0][0], rd[0][1], rd[1][0], rd[1][1]); }
      BLOCK(acc, 0, 0, false, X1L);
    }
    WAIT_LGKM0();
    CBAR();
    f32x4 cs;
    const int fb = (wq == z ? 4096 : 0) + lane * 16;
    const int ao = WS_ABT + tq1 * ABT_ST + (tp1 * 2 + wp) * UB + fb;
    char* own = lds + wp * SLOT + 2 * UB + fb;
#define FO(m, j) (((j) >> 1) * 8192 + ((j) & 1) * 2048 + (m) * 1024)
#define EPI(m, j) do { const u32x4 o = pack8((mb[m][0] * cs[j] + acc[2 * (m)][j]) * rd[m][0], (mb[m][1] * cs[j] + acc[2 * (m) + 1][j]) * rd[m][1]); \
      __builtin_amdgcn_raw_buffer_store_b128(o, rws, ao + FO(m, j), 0, SC1); *reinterpret_cast<u32x4*>(own + FO(m, j)) = o; } while (0)
    static_assert(AT_AT < 15, "");
#define X1E(n) if ((n) == 0) cs = *reinterpret_cast<const f32x4*>(lds + L_CS + (wid ^ 2) * 256 + fr * 16); \
      if ((n) == 4) { cs += f32x4{acs[0][0], acs[1][0], acs[2][0], acs[3][0]}; PIN(cs); } \
      if ((n) == 5) EPI(0, 0); if ((n) == 7) EPI(0, 1); if ((n) == 9) EPI(1, 0); if ((n) == 11) EPI(1, 1); if ((n) == 13) EPI(0, 2); if ((n) == 15) EPI(0, 3)
    BLOCK(acc, 2, 4, false, X1E);
    MWAIT();
    STAMP(2);
    EPI(1, 2); EPI(1, 3);
  }
  f32x4 acb[4][4];
  const int pp = wp * 8192 + lane * 16;
  bool sent = false;
#define FLAG(off, n) do { if (!sent && vm_now() <= (n)) { sent = true; asm volatile("" ::: "memory"); \
    if (lane == 0) __builtin_amdgcn_raw_buffer_store_b32(tag, rws, off, 0, SC1); STAMP(FST); } } while (0)
#define FST 4
  const int f1o = WS_FLAG1 + (tq1 * 16 + tp1) * 128 + wid * 4;
  int qa[3], qb[3], pq[2];
  qa[0] = q0; qb[0] = q1; qa[1] = q0 + SLOT; qb[1] = q1 + SLOT; qa[2] = q0 + 2 * SLOT; qb[2] = q1 + 2 * SLOT; pq[0] = pp + 2 * UB; pq[1] = pp + 2 * SLOT + 2 * UB;
  PIN4(qa[1], qb[1], qa[2], qb[2]); PIN4(pq[0], pq[1], qa[0], qb[0]);
  WAIT_LGKM0();
  CBAR();
  STAMP(5);
#define PQ(i) ((i) < 2 ? pq[0] + (i) * SLOT : pq[1])
#define X2A(n) LDP(n, 0, 3, PQ(s2) + 6144, PQ(s2) + 7168); LDQ(n, 2, 4, qa[s2] + UB + 4096, qb[s2] + UB + 4096); XX(n)
#define X2B(n) LDQ(n, 0, 0, qa[s2], qb[s2]); XX(n)
#define X2C(n) LDQ(n, 0, 4, qa[s2] + 4096, qb[s2] + 4096); XX(n)
#define X2D(n) LDQ(n, 0, 0, qa[s2] + UB, qb[s2] + UB); LDP(n, 4, 0, PQ(s2), PQ(s2) + 1024); LDP(n, 8, 1, PQ(s2) + 2048, PQ(s2) + 3072); \
    LDP(n, 12, 2, PQ(s2) + 4096, PQ(s2) + 5120); XX(n)
#define HEAD2(sl, ZERO) do { constexpr int s2 = sl; BLOCK(acb, 0, 0, ZERO, X2A); BLOCK(acb, 2, 4, ZERO, X2B); BLOCK(acc, 0, 0, ZERO, X2C); } while (0)
#define TAIL2(sn, ZERO) do { constexpr int s2 = sn; WAIT_LGKM0(); CBAR(); BLOCK(acc, 2, 4, ZERO, X2D); } while (0)
#pragma unroll
  for (int n = 0; n < 4; ++n) q[n] = LDF((n & 1 ? qb[0] : qa[0]) + UB + (n >> 1) * 2048);
#pragma unroll
  for (int i = 0; i < 3; ++i) { p[0][i] = LDF(PQ(0) + i * 2048); p[1][i] = LDF(PQ(0) + i * 2048 + 1024); }
  SB();
#define XX(n) if (((n) & 7) == 7) FLAG(f1o, 0)
  HEAD2(0, true); TAIL2(1, true); HEAD2(1, false);
#undef XX
#define XX(n)
  if (!sent) { wait_vm<0>(); FLAG(f1o, 0); }
  u32x4 gf[4];
#define TM(k, l) (*reinterpret_cast<const float*>(lds + L_MB + (wq * 64 + (k) * 16 + ((l) & 15)) * 4))
#define TR(k, l) (*reinterpret_cast<const float*>(lds + L_MB + 512 + (wq * 64 + (k) * 16 + ((l) & 15)) * 4))
#define GATE(k, l) do { const float tr = TR(k, l), tm = TM(k, l) * tr; _Pragma("unroll") for (int c = 0; c < 4; ++c) { \
      const f32x2 n = f32x2{lo2f(xv[k][c]), hi2f(xv[k][c])} * tr + tm, a = n * -1.44269504f; \
      const f32x2 d = f32x2{__builtin_amdgcn_exp2f(a[0]), __builtin_amdgcn_exp2f(a[1])} + 1.0f; \
      const f32x2 g = n * f32x2{__builtin_amdgcn_rcpf(d[0]), __builtin_amdgcn_rcpf(d[1])}; \
      gf[k][c] = pack2(g[0], g[1]); } PIN(gf[k]); PIN(xv[k]); } while (0)
  {
    const int lg = lane_now();
#pragma unroll
    for (int k = 0; k < GF_N; ++k) GATE(k, lg);
  }
  __builtin_amdgcn_sched_barrier(0);
  STAMP(1);
#pragma unroll 1
  for (int n = 0; n < 4; ++n) { TAIL2(2, false); HEAD2(2, false); TAIL2(0, false); HEAD2(0, false); TAIL2(1, false); HEAD2(1, false); }
#define PU(k) ((k) == 0 ? 8 : (k) == 1 ? 7 : (k) == 2 ? 2 : 5)
#define QU(k) ((k) == 0 ? 6 : (k) == 1 ? 0 : (k) == 2 ? 1 : 3)
  const int ln = lane_now();
  const int xw = WS_XCH + wid * XW + ln * 16;
  int p7 = pq[1] - UB;
  PIN(p7);
#define PAD(k) (PU(k) >= 7 ? p7 + (PU(k) - 7) * UB : pq[0] + (PU(k) - 2) * UB)
#define QA0(k) (qa[QU(k) / 3] + QU(k) % 3 * UB)
#define QA1(k) (qb[QU(k) / 3] + QU(k) % 3 * UB)
#define SEND(p, a0, a1, j) __builtin_amdgcn_raw_buffer_store_b128(pack8(a0[j], a1[j]), rws, xw + b * XB + (p) * 4096 + (j) * 1024, 0, SC1)
#define JOB(K, I0, PI, KN, IN, FIRST, EXTRA) do { _Pragma("unroll") for (int n_ = 0; n_ < 16; ++n_) { \
    const int g_ = n_ >> 2, i_ = (g_ ^ n_ >> 1) & 1, h_ = (n_ ^ n_ >> 1) & 1; \
    mm(acc[(I0) + i_][g_], p[h_][(PI) + i_], q[2 * g_ + h_], false); SB(); \
    if (FIRST) { LDQ(n_, 0, 4, QA0(K) + 4096, QA1(K) + 4096); } else if (n_ < 2) q[6 + n_] = LDF((n_ ? QA1(K) : QA0(K)) + 6144); \
    if ((KN) >= 0) { \
      if (n_ >= 4 && (n_ & 3) < 2) q[2 * g_ - 2 + (n_ & 1)] = LDF((n_ & 1 ? QA1(KN) : QA0(KN)) + (g_ - 1) * 2048); \
      LDP(n_, 6, 2 - (PI), PAD(KN) + (IN) * 2048, PAD(KN) + (IN) * 2048 + 1024); LDP(n_, 10, 3 - (PI), PAD(KN) + (IN) * 2048 + 2048, PAD(KN) + (IN) * 2048 + 3072); } \
    if (n_ == 8 && (K) + 2 * (I0) >= RX_FROM) mw = misc[12 + wid]; \
    EXTRA(n_); SB(); } } while (0)
#define X_NONE(n)
#define X_S0(n) if (((n) & 3) == 2) SEND(0, acb[2], acb[3], (n) >> 2)
#define X_S1(n) if (((n) & 3) == 2) SEND(1, acb[0], acb[1], (n) >> 2)
#define LT(i) pack2(acc[((i) >> 1) & 1][((i) >> 2) & 3][((i) & 1) * 2], acc[((i) >> 1) & 1][((i) >> 2) & 3][((i) & 1) * 2 + 1])
#define X_S2(n) if ((n) & 1) { X_S2G((n) >> 1); }
#define X_S2G(g) if ((g) < 5) __builtin_amdgcn_raw_buffer_store_b128(u32x4{LT(3 * (g)), LT(3 * (g) + 1), LT(3 * (g) + 2), tag}, rws, xw + b * XB + 8192 + (g) * 1024, 0, SC1); \
  else if ((g) == 5) __builtin_amdgcn_raw_buffer_store_b64(u32x2{LT(15), tag}, rws, xw + b * XB + 8192 + 5120 - ln * 8, 0, SC1)
#define RECV2() do { RECV(e2, 2, 4); asm volatile("buffer_load_dwordx4 %0, %2, %4, 0 offen sc1\n\tbuffer_load_dwordx2 %1, %3, %4, 0 offen sc1" \
                 : "+&v"(e4), "+&v"(e5) : "v"(xw + (b ^ 4) * XB + 12288), "v"(xw + (b ^ 4) * XB + 8192 + 5120 - ln * 8), "s"(rws) : "memory"); nv += 6; } while (0)
#define FLAGX() do { if (fx < 2 && vm_now() <= nv + (fx ? 0 : 4)) { asm volatile("" ::: "memory"); \
    if (ln == 0) __builtin_amdgcn_raw_buffer_store_b32(tag, rws, xw + b * XB + 13824 + fx * 128, 0, SC1); ++fx; ++nv; } } while (0)
  u32x4 e0[4], e1[4], e2[4], e4; u32x2 e5;
  unsigned fx = 0, nv = 0;
#define RECV(v, p, pb) asm volatile("buffer_load_dwordx4 %0, %4, %5, 0 offen sc1\n\tbuffer_load_dwordx4 %1, %4, %5, 0 offen offset:1024 sc1\n\t" \
    "buffer_load_dwordx4 %2, %4, %5, 0 offen offset:2048 sc1\n\tbuffer_load_dwordx4 %3, %4, %5, 0 offen offset:3072 sc1" \
    : "+&v"(v[0]), "+&v"(v[1]), "+&v"(v[2]), "+&v"(v[3]) : "v"(xw + (b ^ (pb)) * XB + (p) * 4096), "s"(rws) : "memory")
  unsigned mw, got = 0;
#define RX(n) do { FLAGX(); if ((n) < RX_FROM) break; unsigned m_ = rfl(mw) & ~got; if ((n) < 7) m_ &= -m_; \
    if (m_) { got |= m_; if (m_ & 1) { RECV(e0, 0, 2); nv += 4; } if (m_ & 2) { RECV(e1, 1, 6); nv += 4; } if (m_ & 4) RECV2(); } } while (0)
#define X3D(n) LDQ(n, 0, 0, QA0(0), QA1(0)); LDP(n, 4, 0, PAD(0), PAD(0) + 1024); LDP(n, 8, 1, PAD(0) + 2048, PAD(0) + 3072)
  WAIT_LGKM0();
  CBAR();
  BLOCK(acc, 2, 4, false, X3D);
  STAMP(10);
  CBAR();
  JOB(0, 0, 0, 1, 0, true, X_S0);
  JOB(1, 0, 2, 2, 0, false, X_S1);
  FLAGX();
  CBAR();
  JOB(2, 0, 0, 3, 0, false, X_NONE);
  FLAGX();
  JOB(3, 0, 2, 0, 2, false, X_NONE);
  RX(3);
  STAMP(15);
  JOB(0, 2, 0, 1, 2, false, X_S2);
  nv += 6;
  RX(4);
  JOB(1, 2, 2, 2, 2, false, X_NONE);
  RX(5);
  JOB(2, 2, 0, 3, 2, false, X_NONE);
  RX(6);
  JOB(3, 2, 2, -1, 0, false, X_NONE);
  MWAIT();
  STAMP(6);
  RX(7);
#pragma unroll
  for (int k = GF_N; k < 4; ++k) { mw = misc[12 + wid]; GATE(k, ln); RX(8); }
  {
    const u32x4 k0 = *(const LAS u32x4*)(lds + L_MISC + 16), k1 = *(const LAS u32x4*)(lds + L_MISC + 32);
    const int idx = min(0xffff - (int)(rfl(max(max(k0[0], k0[2]), max(k1[0], k1[2]))) & 0xffffu), S - 1);
    if ((idx >> 6) == tp1 * 2 + wq) {
#pragma unroll 1
      for (int spins = 0; spins < SPIN_CAP; ++spins) { if (rfl(misc[16 + wp * 2] & misc[17 + wp * 2])) break; __builtin_amdgcn_s_sleep(2); }
      asm volatile("" ::: "memory");
      const LAS float* upd = (const LAS float*)(lds + L_UPD) + wp * 32 + (ln >> 4) * 8;
#pragma unroll
      for (int k = 0; k < 4; ++k) {
        if (((idx >> 4) & 3) == k && (idx & 15) == (ln & 15)) {
#pragma unroll
          for (int c = 0; c < 4; ++c) {
            const unsigned n = ln_pair(xv[k][c], TM(k, ln), TR(k, ln));
            gf[k][c] = silu_pair(pack2(lo2f(n) + upd[2 * c], hi2f(n) + upd[2 * c + 1]));
          }
        }
      }
    }
  }
#pragma unroll 1
  for (int spins = 0; spins < SPIN_CAP && (got != 7 || fx < 2); ++spins) { mw = misc[12 + wid]; RX(8); }
  STAMP(9);
#pragma unroll 1
  for (int spins = 0; spins < SPIN_CAP; ++spins) {
    wait_vm<0>();
    PIN4(e0[0], e0[1], e0[2], e0[3]); PIN4(e1[0], e1[1], e1[2], e1[3]); PIN4(e2[0], e2[1], e2[2], e2[3]);
    PIN(e4); PIN(e5);
    if (__all(((e2[0][3] ^ tag) | (e2[1][3] ^ tag) | (e2[2][3] ^ tag) | (e2[3][3] ^ tag) | (e4[3] ^ tag) | (e5[1] ^ tag)) == 0)) break;
    RECV2();
  }
  STAMP(3);
  const rsrc_t rout = mk_rsrc((const void*)pout, S * D * 2);
#pragma unroll
  for (int k = 0; k < 4; ++k) {
    u32x4 ov;
#pragma unroll
    for (int c = 0; c < 4; ++c) {
      const unsigned a1 = e0[k][c], a2 = e1[k][c], a3 = 4 * k + c < 12 ? e2[(4 * k + c) / 3 & 3][(4 * k + c) % 3] : 4 * k + c < 15 ? e4[(4 * k + c) % 3] : e5[0];
      const f32x2 sv = (f32x2{acc[2 + (c >> 1)][k][(c & 1) * 2], acc[2 + (c >> 1)][k][(c & 1) * 2 + 1]} + f32x2{lo2f(a1), hi2f(a1)}) +
                       (f32x2{lo2f(a2), hi2f(a2)} + f32x2{lo2f(a3), hi2f(a3)});
      const f32x2 r = sv * f32x2{lo2f(gf[k][c]), hi2f(gf[k][c])} + f32x2{lo2f(xv[k][c]), hi2f(xv[k][c])};
      ov[c] = pack2(r[0], r[1]);
    }
    __builtin_amdgcn_raw_buffer_store_b128(ov, rout, XO(ln) + k * 16 * D * 2, 0, SC1);
  }
  STAMP(8);
  STAMP_OUT();
}

__global__ __launch_bounds__(512, 2) void fused(const u16* __restrict__ x, const u16* __restrict__ sre, const u16* __restrict__ sim,
                                                const u16* __restrict__ hre, const u16* __restrict__ him, const u16* __restrict__ wr,
                                                const u16* __restrict__ wg, const u16* __restrict__ wu, const u16* __restrict__ wdn,
                                                char* ws, u16* out) {
  extern __shared__ __attribute__((aligned(16))) char lds[];
  const karg_t ka = (karg_t)__builtin_amdgcn_kernarg_segment_ptr();
  u64 px, pre, pim;
  asm volatile("s_load_dwordx2 %0, %3, 0\n\ts_load_dwordx2 %1, %3, 24\n\ts_load_dwordx2 %2, %3, 32" : "=&s"(px), "=&s"(pre), "=&s"(pim) : "s"(ka));
  __builtin_amdgcn_sched_barrier(0);
  const int wid = __builtin_amdgcn_readfirstlane(threadIdx.x >> 6), lane = threadIdx.x & 63;
  const int b = blockIdx.x, g = b & 7, i = b >> 3;
  const int tp1 = (g & 3) * 4 + (i & 3), tq1 = (g >> 2) * 8 + (i >> 2);
  unsigned tch = 0;
  u64 pc;
  asm volatile("s_getpc_b64 %0" : "=s"(pc));
  pc &= ~255ull;
  if (wid == 3) {
    asm volatile("global_load_dword %0, %1, %2" : "+v"(tch) : "v"(min((i & 7) + lane * 8, CODE_LINES - 1) * 128), "s"(pc) : "memory");
  }
  if (__builtin_expect(wid >= 4, 1)) service(wid & 3, lane, b, lds, ka, px, pre, pim, pc, tp1, tq1 STAMP_PASS);
  else {
    asm volatile("s_waitcnt lgkmcnt(0)" :: "s"(px), "s"(pre), "s"(pim));
    consumer(wid, lane, b, lds, ka, tch, tp1, tq1 STAMP_PASS);
  }
}

extern "C" void kernel_launch(void* const* d_in, const int* in_sizes, int n_in, void* d_out, int out_size,
                              void* d_ws, size_t ws_size, hipStream_t stream) {
  (void)hipFuncSetAttribute((const void*)fused, hipFuncAttributeMaxDynamicSharedMemorySize, LDS_BYTES);
  fused<<<256, 512, LDS_BYTES, stream>>>((const u16*)d_in[0], (const u16*)d_in[1], (const u16*)d_in[2], (const u16*)d_in[3],
                                         (const u16*)d_in[4], (const u16*)d_in[6], (const u16*)d_in[7], (const u16*)d_in[8],
                                         (const u16*)d_in[9], (char*)d_ws, (u16*)d_out);
}
```

```cpp
#define CODE_LINES 279
#include <hip/hip_runtime.h>
#include <hip/hip_bf16.h>
#include <stdint.h>
#include <utility>

typedef __attribute__((ext_vector_type(8))) short bf16x8;
typedef __attribute__((ext_vector_type(4))) float f32x4;
typedef __attribute__((ext_vector_type(2))) float f32x2;
typedef __attribute__((ext_vector_type(4))) unsigned u32x4;
typedef __attribute__((ext_vector_type(2))) unsigned u32x2;
typedef __attribute__((ext_vector_type(2))) __bf16 bf16x2;
typedef unsigned short u16;
typedef unsigned long long u64;
typedef __amdgpu_buffer_rsrc_t rsrc_t;
#define LAS __attribute__((address_space(3)))

constexpr int S = 2048, D = 1024, F = 3584;
constexpr int UB = 16384;
constexpr int SLOT = 3 * UB;
constexpr int L_MISC = 9 * UB;
constexpr int L_UPD = L_MISC + 256;
constexpr int L_MB = L_MISC + 512;
constexpr int L_CS = L_MB + 1024;
constexpr int L_CMB = L_CS + 1024;
constexpr int L_DBG = L_CMB + 2048;
constexpr int LDS_BYTES = L_DBG + 512;
constexpr int L_ST = 6 * UB;
constexpr int NBAR = 17 + 14 + 3;
constexpr int SC1 = 16, NT = 2;
constexpr int OOB = 0x7ffffff0;

constexpr unsigned WS_EPOCH = 0;
constexpr unsigned WS_STAT = 4096;
constexpr unsigned WS_AMAX = WS_STAT + S * 8;
constexpr unsigned WS_FLAG1 = WS_AMAX + 4096;
constexpr unsigned WS_GU = WS_FLAG1 + 32768;
constexpr unsigned WS_PART = WS_GU + 4 * 2 * F * 8;
constexpr unsigned WS_XCH = WS_PART + 32 * D * 8;
constexpr int XW = 14336;
constexpr int XB = 4 * XW;
constexpr unsigned ABT_ST = 128 * S * 2;
constexpr unsigned WS_ABT = WS_XCH + 256 * XB;
constexpr unsigned WS_END = WS_ABT + 16 * ABT_ST;

#define STAMP(i)
#define CBAR() BAR()
#define STAMP_DECL
#define STAMP_ARG
#define STAMP_PASS
#define STAMP_OUT()

__device__ __forceinline__ float lo2f(unsigned v) { return __uint_as_float(v << 16); }
__device__ __forceinline__ float hi2f(unsigned v) { return __uint_as_float(v & 0xffff0000u); }
__device__ __forceinline__ unsigned pack2(float a, float b) {
  return __builtin_bit_cast(unsigned, __builtin_convertvector(f32x2{a, b}, bf16x2));
}
__device__ __forceinline__ float rbf(float f) { return lo2f(pack2(f, f)); }
__device__ __forceinline__ unsigned rfl(unsigned v) { return __builtin_amdgcn_readfirstlane(v); }
__device__ __forceinline__ rsrc_t mk_rsrc(const void* p, unsigned bytes) {
  return __builtin_amdgcn_make_buffer_rsrc((void*)p, 0, bytes, 0x00020000);
}
#define DPP_ADD(v, ctrl, rm, bc) v += __builtin_bit_cast(float, __builtin_amdgcn_update_dpp(0, __builtin_bit_cast(int, v), ctrl, rm, 0xf, bc))
#define DPP_MAXU(v, ctrl, rm, bc) v = max(v, (unsigned)__builtin_amdgcn_update_dpp(0, (int)v, ctrl, rm, 0xf, bc))
__device__ __forceinline__ unsigned wave_maxu(unsigned v) {
  DPP_MAXU(v, 0x111, 0xf, true); DPP_MAXU(v, 0x112, 0xf, true); DPP_MAXU(v, 0x114, 0xf, true); DPP_MAXU(v, 0x118, 0xf, true);
  DPP_MAXU(v, 0x142, 0xa, false); DPP_MAXU(v, 0x143, 0xc, false);
  return (unsigned)__builtin_amdgcn_readlane((int)v, 63);
}
template <int N> __device__ __forceinline__ void wait_vm() { asm volatile("s_waitcnt vmcnt(%0)" :: "n"(N) : "memory"); }
#define WAIT_LGKM0() do { asm volatile("" ::: "memory"); __builtin_amdgcn_s_waitcnt(0xc07f); asm volatile("" ::: "memory"); } while (0)
#define BAR() do { asm volatile("" ::: "memory"); __builtin_amdgcn_s_barrier(); asm volatile("" ::: "memory"); } while (0)
#define MFMA(a, b, c) __builtin_amdgcn_mfma_f32_16x16x32_bf16(a, b, c, 0, 0, 0)
#define BF8(v) __builtin_bit_cast(bf16x8, v)
#define PIN(a) asm volatile("" : "+v"(a))
#define PIN4(a, b, c, d) asm volatile("" : "+v"(a), "+v"(b), "+v"(c), "+v"(d))
constexpr int SPIN_CAP = 1 << 20;
__device__ __forceinline__ unsigned vm_now() { const unsigned r = __builtin_amdgcn_s_getreg(7 | (31 << 11)); return (r & 15u) | ((r >> 18) & 0x30u); }
__device__ __forceinline__ unsigned mk_tag(unsigned epoch) { return rfl(epoch) * 0x9E3779B1u + 0x7F4A7C15u; }
__device__ __forceinline__ float silu_fast(float z) { return z * __builtin_amdgcn_rcpf(1.0f + __expf(-z)); }
__device__ __forceinline__ u32x4 pack8(const f32x4& a, const f32x4& c) {
  return u32x4{pack2(a[0], a[1]), pack2(a[2], a[3]), pack2(c[0], c[1]), pack2(c[2], c[3])};
}
__device__ __forceinline__ unsigned ln_pair(unsigned x, float nmb, float rden) {
  const unsigned d = pack2(lo2f(x) + nmb, hi2f(x) + nmb);
  return pack2(lo2f(d) * rden, hi2f(d) * rden);
}
#define ADD2_DPP(a, b, mod) asm volatile("s_nop 1\n\tv_add_f32_dpp %0, %0, %0 " mod " row_mask:0xf bank_mask:0xf\n\tv_add_f32_dpp %1, %1, %1 " mod " row_mask:0xf bank_mask:0xf" : "+v"(a), "+v"(b))
#define ALLEQ(a, t) (__builtin_amdgcn_ballot_w64((a) == (t)))
__device__ __forceinline__ unsigned silu_pair(unsigned n) { return pack2(silu_fast(lo2f(n)), silu_fast(hi2f(n))); }
__device__ __forceinline__ int lane_now() { int l; asm volatile("v_mbcnt_lo_u32_b32 %0, -1, 0\n\tv_mbcnt_hi_u32_b32 %0, -1, %0" : "=v"(l)); return l; }

#define PRE_H 1
#define CT2_AT 18
#define CT3_AT 28
#define NEED_TO 23
#define SC_AT 3
#define PT_AT 3
#define XN_AT 17
#define SET2_AT 16
#define AM_AT 12
#define AT_AT 14
#define Q0_AT 14
#define Q1_AT 15
#define CGU_AT 19
#define GG_AT 21
#define GD_AT 23
#define GE_AT 24
constexpr int XE_AT = AT_AT + 1, Q2_AT = 17;
#define H_SCHED      {2,2,3,3,2,2,2,2,2,2,2,2,2,2,0,0,0,0,0,0,0,0,0,0,0,0,0,0,0,0,0,0,0,0,0}
#define V_SCHED      {0,0,0,0,0,0,0,0,0,0,0,0,0,0,0,0,0,0,8,0,0,0,0,0,0,0,0,0,0,0,0,0,0,0,0}
#define W_SCHED      {0,0,0,0,3,0,0,3,0,0,3,0,0,4,3,0,0,0,0,0,0,0,7,0,0,0,0,0,0,0,0,0,0,0,0}
#define T_SCHED      {0,0,4,4,4,4,4,4,4,4,4,4,4,4,4,0,0,0,0,0,0,0,0,0,0,0,0,0,0,0,0,0,0,0,0}
#define U_SCHED      {0,0,0,0,0,0,0,0,0,0,0,0,0,0,0,0,0,0,0,0,44,0,0,0,0,32,0,0,0,0,0,0,0,0,0}
#define GF_N 2
#define RX_FROM 6
#define KROT(tp1) ((((tp1) & 12) + (tq1 & 8) + ((tq1 ^ (tp1)) & 1)) & 15)
typedef const __attribute__((address_space(4))) u64* karg_t;

#define DMA4(POL, r, v0, v1, v2, v3, so, dst) do { \
    asm volatile("s_mov_b32 m0, %6\n\ts_nop 0\n\tbuffer_load_dwordx4 %0, %4, %5 offen" POL " lds\n\t" \
                 "buffer_load_dwordx4 %1, %4, %5 offen offset:1024" POL " lds\n\tbuffer_load_dwordx4 %2, %4, %5 offen offset:2048" POL " lds\n\t" \
                 "buffer_load_dwordx4 %3, %4, %5 offen offset:3072" POL " lds" \
                 :: "v"(v0), "v"(v1), "v"(v2), "v"(v3), "s"(r), "s"(so), "s"(dst) : "memory"); } while (0)
#define LD4Z(POL, d, r, vo) do { asm volatile("buffer_load_dwordx4 %0, %1, %2, 0 offen" POL : "=v"(d) : "v"(vo), "s"(r) : "memory"); ++ops; } while (0)
#define TOUCH(POL, r, vo) do { asm volatile("buffer_load_dword %0, %1, %2, 0 offen" POL : "+v"(tch) : "v"(vo), "s"(r) : "memory"); ++ops; } while (0)
#define ST4(d, vo) asm volatile("buffer_store_dwordx4 %0, %1, %2, 0 offen sc1\n\ts_nop 1" :: "v"(d), "v"(vo), "s"(rws) : "memory")
#define ST1(d, vo) asm volatile("buffer_store_dword %0, %1, %2, 0 offen sc1" :: "v"(d), "v"(vo), "s"(rws) : "memory")
struct Cnt {
  int x, y0, y3;
  __device__ __forceinline__ void operator++() { ++x; }
  __device__ __forceinline__ void operator+=(int n) { x += n; }
};
__device__ __forceinline__ void wait_n(int n) { asm volatile("s_waitcnt vmcnt(%0)" :: "n"(n > 63 ? 63 : n) : "memory"); }
__device__ __forceinline__ void wait_mark(Cnt ops, Cnt m, int v) {
  const int d = ops.x - m.x, e0 = ops.y0 - m.y0, e3 = ops.y3 - m.y3;
  if (d >= 63) wait_n(63);
  else if (e0 && v == 0) wait_n(d + e0);
  else if (e3 && v == 3) wait_n(d + e3);
  else wait_n(d);
}
#define WAITM(m) wait_mark(ops, m, v)

__device__ __forceinline__ f32x2 add2(f32x4 e, f32x4 o) { float a = e[0] + o[2]; PIN(a); return f32x2{a, e[1] + o[3]}; }
__device__ __forceinline__ void ldw(u32x4& d, rsrc_t r, int vo, int so) { asm volatile("buffer_load_dwordx4 %0, %1, %2, %3 offen nt" : "=v"(d) : "v"(vo), "s"(r), "s"(so) : "memory"); }
__device__ __forceinline__ void stouch(u64& sd, const char* p, int off) { asm volatile("s_load_dwordx2 %0, %1, %2" : "+s"(sd) : "s"(p), "n"(off) : "memory"); }
constexpr int usched[] = U_SCHED, wsched[] = W_SCHED, tsched[] = T_SCHED, hsched[] = H_SCHED, vsched[] = V_SCHED;
constexpr int prefix(const int* a, int n) { int s = 0; for (int i = 0; i < n; ++i) s += a[i]; return s; }
static_assert(prefix(wsched, NBAR + 1) == 23 && prefix(tsched, NBAR + 1) + prefix(usched, NBAR + 1) == 128 && prefix(hsched, NBAR + 1) == 30 && prefix(vsched, NBAR + 1) == 8, "");
constexpr int useq(int i) { for (int s = -1; s < NBAR; ++s) if (usched[s + 1] && i-- == 0) return s; return NBAR; }
constexpr int uidx(int sl) { int n = 0; for (int s = -1; s < sl; ++s) n += usched[s + 1] > 0; return n; }
static_assert(uidx(NBAR) <= 4, "");
template <class Fn, int... I>
__device__ __forceinline__ void static_for(Fn& fn, std::integer_sequence<int, I...>) { (fn(std::integral_constant<int, I>{}), ...); }

__device__ __forceinline__ void service(int v, int lane, int b, char* lds, karg_t ka, u64 px, u64 pre, u64 pim, u64 pc, int tp1, int tq1 STAMP_ARG) {
  STAMP_DECL;
  volatile LAS unsigned* misc = (volatile LAS unsigned*)(lds + L_MISC);
  const unsigned l0 = (unsigned)(uintptr_t)(LAS char*)lds;
  Cnt ops = {0, 0, 0}, m1[16], m2[14], m3[4], mam, mxe, mw[23], mgg, mp2;
  int va0 = ((lane >> 5) * 8 + (lane >> 3 & 3)) * (D * 2) + ((lane ^ lane >> 3) & 7) * 16;
  int va1 = va0 + 16 * (D * 2) - 1024, va2 = va0 + 4 * (D * 2) - 2048, va3 = va0 + 20 * (D * 2) - 3072;
  const unsigned utab = v >> 1 ? 0x8541u : 0x7230u;
  int k0 = KROT(tp1) * 128;
  unsigned g1o = ((v < 2 ? tp1 : tq1 & 7) * 128 + (v & 1) * 64) * (D * 2), ld1 = l0 + (v & 1) * 8192;
  asm volatile("s_waitcnt lgkmcnt(0)" : "+s"(px), "+s"(pre), "+s"(pim), "+s"(g1o), "+s"(k0), "+s"(ld1), "+v"(va0), "+v"(va1), "+v"(va2), "+v"(va3));
  const rsrc_t r1 = mk_rsrc((const char*)(v < 2 ? px : tq1 & 8 ? pim : pre) + g1o, 64 * D * 2);
#define HALF1(n) do { DMA4("", r1, va0, va1, va2, va3, ((((n) >> 1) * 128 + k0) & 2047) + ((n) & 1) * 32 * D * 2, \
    ld1 + ((utab >> ((((n) >> 1) & 3) * 4)) & 15) * UB + ((n) & 1) * 4096); ops += 4; m1[(n) >> 1] = ops; } while (0)
#pragma unroll
  for (int n = 0; n < 2 + hsched[0]; ++n) HALF1(n);
  STAMP(1);
  asm volatile("" : "+s"(b), "+s"(v), "+s"(tp1), "+s"(tq1), "+v"(lane));
  __builtin_amdgcn_sched_barrier(0);
  __builtin_assume(lane >= 0 && lane < 64 && b >= 0 && b < 256 && v >= 0 && v < 4 && tp1 >= 0 && tp1 < 16 && tq1 >= 0 && tq1 < 16);
  const int rsub = lane >> 3, ch = ((lane & 7) ^ rsub) * 16;
  const int lc = lane & 15, lq = lane >> 4;
  const int z = tq1 >> 3, te = tq1 & 7, h = tp1 >> 3;
  u64 ktab;
  {
    const unsigned ta = (tp1 & 2) ? ((tp1 & 1) ? 0x2107653u : 0x3107652u) : ((tp1 & 1) ? 0x3207651u : 0x3217650u);
    ktab = ((u64)((tp1 & 1) ? 0x4cu : 0xc4u) << 28 | ta) ^ (0x111111111ull * (unsigned)(tp1 & 12));
  }
  const int kfl = tp1 & 1;
#define KTILE(m) ((int)(ktab >> ((m) * 4) & 15) * 2)
#define KSTEP(n) (KTILE((n) >> 1) + ((n) < 2 ? (n) & 1 : ((n) & 1) ^ kfl))
  int vq0 = rsub * (S * 2) + ch, vq1 = vq0 + 8 * S * 2 - 1024, vq2 = vq0 + 16 * S * 2 - 2048, vq3 = vq0 + 24 * S * 2 - 3072;
  int vp = lane * 16;
  u64 sd = 0;
  const int qa = (((v >> 1) ^ h) * 1024 + (v & 1) * 64) * (S * 2), qb = (h * 1024 + v * 32) * (S * 2);
#define QBIG(n) do { const int k_ = qa + KSTEP(n) * 128; const unsigned d_ = l0 + ((n) % 3) * SLOT + v * 8192; \
    DMA4("", rq, vq0, vq1, vq2, vq3, k_, d_); DMA4("", rq, vq0, vq1, vq2, vq3, k_ + 32 * S * 2, d_ + 4096); ops += 8; } while (0)
#define PIMG(n, u, q) DMA4(" sc1", rp, vp, vp, vp, vp, KSTEP(n) * UB + (q) * 4096, l0 + (u) * UB + (q) * 4096)
#define QS(j, u) do { DMA4("", rq, vq0, vq1, vq2, vq3, qb + KSTEP(14 + (j)) * 128, l0 + (u) * UB + v * 4096); ops += 4; m3[j] = ops; } while (0)
#define PS(j, u) do { PIMG(14 + (j), u, v); ops += 4; m3[j] = ops; } while (0)
  const int bf = b ^ 3, rgm = bf * 2341 >> 16, gm = rgm & 1, rg = rgm >> 1, cg = bf - rgm * 28, fg = bf >> 3, eg = bf & 7;
  int vgu = lq * 16 * F * 2 + lc * 16;
  f32x4 ve = {0.f, 0.f, 0.f, 0.f}, vo = ve;
  u32x4 w[16], wd[8], tb, am, fin[4];
  unsigned tag, tch = 0, xe, key, sg, fl, pl;
  int idx;
  unsigned mask = 0;
  unsigned hp, xf;
  u32x4 da = {0u, 0u, 0u, 0u}, db = da;
  u32x2 gg[4];
#define DG1(a, t, k, d) a[d] = (unsigned)__builtin_amdgcn_update_dpp((int)a[d], (int)t, (d) == (k) ? 0xe4 : 0x120 + (4 * ((d) - (k)) & 15), 0xf, 1 << (d), false)
#define DIAG(a, t, k) do { DG1(a, t, k, 0); DG1(a, t, k, 1); DG1(a, t, k, 2); DG1(a, t, k, 3); } while (0)
  int fo = WS_FLAG1 + tq1 * 2048 + (int)(ktab >> (4 + (lane & 28)) & 15) * 128 + (lane & 3) * 4;
#define NEED(need) do { mask |= (unsigned)ALLEQ(fl, tag); if (__builtin_expect((mask & (need)) != (need), 0)) \
    _Pragma("unroll 1") for (int spins = 0; (mask & (need)) != (need) && spins < SPIN_CAP; ++spins) { \
      asm volatile("buffer_load_dword %0, %1, %2, 0 offen sc1\n\ts_waitcnt vmcnt(0)" : "+v"(fl) : "v"(fo), "s"(rws) : "memory"); \
      mask |= (unsigned)ALLEQ(fl, tag); } } while (0)
#define GGLD() asm volatile("buffer_load_dwordx2 %0, %4, %5, 0 offen sc1\n\tbuffer_load_dwordx2 %1, %4, %5, %6 offen sc1\n\t" \
                     "buffer_load_dwordx2 %2, %4, %5, %7 offen sc1\n\tbuffer_load_dwordx2 %3, %4, %5, %8 offen sc1" \
                     : "+&v"(gg[0]), "+&v"(gg[1]), "+&v"(gg[2]), "+&v"(gg[3]) : "v"(guo), "s"(rws), "s"(F * 8), "s"(4 * F * 8), "s"(5 * F * 8) : "memory")
#define FINLD() asm volatile("buffer_load_dwordx4 %0, %4, %5, 0 offen sc1\n\tbuffer_load_dwordx4 %1, %4, %5, %6 offen sc1\n\t" \
                     "buffer_load_dwordx4 %2, %4, %5, %7 offen sc1\n\tbuffer_load_dwordx4 %3, %4, %5, %8 offen sc1" \
                     : "+&v"(fin[0]), "+&v"(fin[1]), "+&v"(fin[2]), "+&v"(fin[3]) : "v"(fgo), "s"(rws), "s"(8 * D * 8), "s"(16 * D * 8), "s"(24 * D * 8) : "memory")
  auto final_sum = [&]() __attribute__((always_inline)) {
      float y0 = (__uint_as_float(fin[0][0]) + __uint_as_float(fin[1][0])) + (__uint_as_float(fin[2][0]) + __uint_as_float(fin[3][0]));
      float y1 = (__uint_as_float(fin[0][2]) + __uint_as_float(fin[1][2])) + (__uint_as_float(fin[2][2]) + __uint_as_float(fin[3][2]));
      DPP_ADD(y0, 0x128, 0xf, false); DPP_ADD(y1, 0x128, 0xf, false);
      y0 += __shfl_xor(y0, 16); y1 += __shfl_xor(y1, 16);
      y0 += __shfl_xor(y0, 32); y1 += __shfl_xor(y1, 32);
      const unsigned srt = key >> 16;
      const float r = lo2f((srt & 0x8000u) ? (srt & 0x7fffu) : (~srt & 0xffffu));
      const float rsel = rbf(1.0f / (1.0f + __expf(-r)));
      *(LAS f32x2*)(lds + L_UPD + v * 64 + (lane & 7) * 8) = f32x2{rbf(rbf(y0) * rsel), rbf(rbf(y1) * rsel)};
      WAIT_LGKM0();
      if (lane == 0) misc[16 + v] = 1u;
      STAMP(10);
  };
  int tbo = WS_STAT + tp1 * 1024 + vp, amo = WS_AMAX + v * 1024 + vp;
  int pt0 = WS_XCH + b * XB + v * XW + 13824, pt1 = WS_FLAG1 + (tq1 * 16 + tp1) * 128;
  asm volatile("" : "+s"(pt0), "+s"(pt1));
  int pto = lane < 2 ? pt0 + lane * 128 : pt1;
  int cmo = L_CMB + v * 512 + (lc * 4 + lq) * 8, xeo = ((rg & 3) * 256 + v * 64 + lq * 16 + (lc >> 1)) * 2, l8 = lane * 8;
  int fo1 = WS_FLAG1 + tq1 * 2048 + (KSTEP(2) >> 1) * 128 + (lane & 3) * 4, amp = lane == 4 ? WS_AMAX + b * 16 : lane < 4 ? WS_STAT + (tp1 * 128 + tq1 * 8) * 8 + vp : OOB;
  PIN4(tbo, amo, fo, fo1); PIN4(pto, cmo, xeo, l8); PIN4(vq0, vq1, vq2, vq3); PIN4(vgu, vp, da, db); PIN(amp);
  int guo, fgo, puo, pdo, co, vdn, vdl, uo[4];
  unsigned hmask;
  auto setup2 = [&]() __attribute__((always_inline)) {
    const int l = lane_now(), c = l & 15, q = l >> 4;
    __builtin_assume(l >= 0 && l < 64);
    guo = WS_GU + ((c & 1) * 2 * F + fg * 112 + v * 28 + (c < 12 ? (c & 12) * 2 + q * 2 + (c >> 1 & 1) : 24 + q)) * 8;
    fgo = WS_PART + ((l >> 3) * D + te * 128 + z * 64 + v * 16 + (l & 7) * 2) * 8;
    puo = (b < 224 ? WS_GU + bf * 1024 : OOB - 1024) + l * 16; pdo = WS_PART + bf * 1024 + l * 16;
    co = WS_XCH + (b ^ (0x462 >> l * 4 & 15)) * XB + v * XW + (l == 2 ? 12288 + 4 : 13824 + l * 128);
    PIN(co);
    co = l < 3 ? co : OOB;
    vdn = q * 2 * D * 2 + c * 16; vdl = (24 + q) * D * 2 + c * 16;
    hmask = l & 1 ? 0xffff0000u : 0xffffu;
#define UOFF(i) if constexpr (useq(i) < NBAR) { constexpr int sl = useq(i), cnt = usched[sl < NBAR ? sl + 1 : 0], n0 = prefix(tsched, NBAR + 1) + prefix(usched, sl + 1); \
      static_assert(sl > 18, ""); const int n = n0 + min(l, cnt - 1), m = n < 112 ? n >> 4 : (n >> 3) - 7, r = n < 112 ? n & 15 : n & 7; \
      const int ra = (te >> 2) * 1024 + ((te & 3) * 4 + v) * 8, rb = h * 1024 + (te * 4 + v) * 4; \
      uo[i] = ((n < 112 ? ra : rb) + (r >> 1)) * (S * 2) + ((int)(ktab >> (m * 4) & 15) * 2 + (r & 1)) * 128; PIN(uo[i]); }
    UOFF(0) UOFF(1) UOFF(2) UOFF(3)
    PIN4(guo, fgo, puo, pdo); PIN4(co, vdn, vdl, hmask);
  };
  if (v == 3 && lane < 8) misc[12 + lane] = 0u;
  WAITM(m1[0]);
  BAR();
#pragma unroll
  for (int n = 2 + hsched[0]; n < 2 + hsched[0] + PRE_H; ++n) HALF1(n);
  asm volatile("" : "+s"(ka));
  __builtin_amdgcn_sched_barrier(0);
  const char* ws = (const char*)ka[9];
  const char* gq = (const char*)ka[1 + z] + (size_t)(tp1 & 7) * 128 * S * 2;
  const rsrc_t rws = mk_rsrc(ws, WS_END), rx = mk_rsrc((const void*)px, S * D * 2);
  const rsrc_t rq = mk_rsrc(gq, 1152 * S * 2), rp = mk_rsrc(ws + WS_ABT + (size_t)tq1 * ABT_ST, 128 * S * 2);
  const rsrc_t rgu = mk_rsrc((const u16*)ka[6 + gm] + (size_t)((rg & 3) * 256 + v * 64) * F + cg * 128, b < 224 ? 64 * F * 2 : 0);
  const rsrc_t rd = mk_rsrc((const u16*)ka[8] + (size_t)(fg * 112 + v * 28) * D + eg * 128, 28 * D * 2);
  const char* ga_ = gq + (size_t)((te >> 2) * 1024 + ((te & 3) * 4 + v) * 8) * (S * 2);
  const char* gb_ = gq + (size_t)(h * 1024 + (te * 4 + v) * 4) * (S * 2);
  asm volatile("" :: "s"(rws), "s"(rq), "s"(rp), "s"(rgu), "s"(rd), "s"(ga_));
  WAIT_LGKM0();
#define UNTIL(pin, ok, reload) do { pin; if (__builtin_expect(!(ok), 0)) { _Pragma("unroll 1") for (int spins = 0; spins < SPIN_CAP; ++spins) { \
      __builtin_amdgcn_s_sleep(2); reload; wait_n(0); pin; if (ok) break; } } } while (0)
  auto work = [&](auto slc) __attribute__((always_inline)) {
    constexpr int sl = decltype(slc)::value - 1;
    if (sl == AT_AT) {
      WAITM(mam);
      UNTIL(asm volatile("" : "+v"(tb), "+v"(am)), (ALLEQ(tb[1], tag) & ALLEQ(tb[3], tag) & ALLEQ(am[1], tag) & ALLEQ(am[3], tag)) == ~0ull,
            asm volatile("buffer_load_dwordx4 %0, %2, %4, 0 offen sc1\n\tbuffer_load_dwordx4 %1, %3, %4, 0 offen sc1" : "+v"(tb), "+v"(am) : "v"(tbo), "v"(amo), "s"(rws) : "memory"));
      if (v == 0) *(LAS u32x2*)(lds + L_MB + l8) = u32x2{tb[0] << 16 ^ 0x80000000u, tb[2] << 16 ^ 0x80000000u};
      if (v == 1) *(LAS f32x2*)(lds + L_MB + 512 + l8) = f32x2{__builtin_amdgcn_rcpf(hi2f(tb[0])), __builtin_amdgcn_rcpf(hi2f(tb[2]))};
      const unsigned mk = wave_maxu(am[0]);
      if (am[0] == mk) *(LAS u32x2*)(lds + L_MISC + 16 + v * 8) = u32x2{mk, am[2]};
    }
    if (sl == XN_AT) {
      WAITM(mxe);
      PIN(xe); PIN(xf);
      const unsigned n_ = ln_pair(xe | xf << 16, -lo2f(sg), __builtin_amdgcn_rcpf(hi2f(sg)));
      xe = lane & 1 ? n_ << 16 : n_ & 0xffffu; xf = lane & 1 ? n_ & 0xffff0000u : n_ >> 16;
      PIN(xe); PIN(xf);
    }
    constexpr int v0 = prefix(vsched, sl + 1), vn = vsched[sl + 1];
    if constexpr (vn > 0) {
      static_assert(prefix(wsched, sl + 1) >= 2 * (v0 + vn) && sl >= XN_AT && sl < CGU_AT, "");
      WAITM(mw[2 * (v0 + vn) - 1]);
#define VPAIR(k) if ((k) >= v0 && (k) < v0 + vn) { PIN(w[2 * (k)]); PIN(w[2 * (k) + 1]); \
        if ((k) & 1) DIAG(db, ((k) < 4 ? xe : xf), (k) & 3); else DIAG(da, ((k) < 4 ? xe : xf), (k) & 3); \
        ve = MFMA(BF8((k) & 1 ? db : da), BF8(w[2 * (k)]), ve); vo = MFMA(BF8((k) & 1 ? db : da), BF8(w[2 * (k) + 1]), vo); }
      VPAIR(0) VPAIR(1) VPAIR(2) VPAIR(3) VPAIR(4) VPAIR(5) VPAIR(6) VPAIR(7)
      if (v0 + vn == 8) *(LAS f32x2*)(lds + cmo) = add2(ve, vo);
      else asm volatile("" : "+v"(ve), "+v"(vo));
    }
    if (sl == GD_AT) {
      WAITM(mgg);
      UNTIL(PIN4(gg[0], gg[1], gg[2], gg[3]), (ALLEQ(gg[0][1], tag) & ALLEQ(gg[1][1], tag) & ALLEQ(gg[2][1], tag) & ALLEQ(gg[3][1], tag)) == ~0ull, GGLD());
      float g_ = __uint_as_float(gg[0][0]) + __uint_as_float(gg[2][0]);
      PIN(g_);
      float u_ = __uint_as_float(gg[1][0]) + __uint_as_float(gg[3][0]);
      ADD2_DPP(g_, u_, "quad_perm:[1,0,3,2]");
      const unsigned gu = pack2(g_, u_);
      const float h_ = rbf(silu_fast(lo2f(gu))) * hi2f(gu);
      hp = pack2(h_, h_) & hmask;
      PIN(hp);
    }
    if (sl == GE_AT) {
      static_assert(prefix(wsched, GE_AT + 1) == 23 && GE_AT >= GD_AT, "");
      WAITM(mw[22]);
      PIN4(wd[0], wd[1], wd[2], wd[3]); PIN4(wd[3], wd[4], wd[5], wd[6]);
      f32x4 ae = {0.f, 0.f, 0.f, 0.f}, ao = ae;
#define DPAIR(i) { if ((i) & 1) DIAG(db, hp, i); else DIAG(da, hp, i); \
        ae = MFMA(BF8((i) & 1 ? db : da), BF8(wd[2 * (i)]), ae); if ((i) < 3) ao = MFMA(BF8((i) & 1 ? db : da), BF8(wd[2 * (i) + 1]), ao); }
      DPAIR(0) DPAIR(1) DPAIR(2) DPAIR(3)
      PIN(ae); PIN(ao);
      *(LAS f32x2*)(lds + cmo) = add2(ae, ao);
    }
  };
  auto slot = [&](auto slc) __attribute__((always_inline)) {
    constexpr int sl = decltype(slc)::value - 1, nw = prefix(wsched, sl + 1), nt = prefix(tsched, sl + 1);
    if constexpr (sl > 0) {
      if constexpr (sl < 16) WAITM(m1[sl]);
      else if constexpr (sl == 19) { if (v == 0) WAITM(mp2); else WAITM(m2[2]); }
      else if constexpr (sl >= 17 && sl < 31) WAITM(m2[sl - 17]);
      else if constexpr (sl >= 31) WAITM(m3[sl == 31 ? 0 : sl - 30]);
      if (sl == 19 && v == 0) { PIN(fl); NEED(0xf0u); }
      if constexpr (sl >= 21 && sl <= NEED_TO && (sl & 1)) { if (v == 0) { PIN(fl); NEED(sl == NEED_TO ? ~0u : 0xfu << 2 * (sl - 17)); } }
      BAR();
    }
    u32x4 pub;
    constexpr bool cgu = sl == CGU_AT, cdn = sl == GE_AT + 1;
    if (sl == SC_AT) {
      if (v == 3) {
        tag = rfl(misc[2]);
        const u32x4 k0 = *(const LAS u32x4*)(lds + L_MISC + 80), k1 = *(const LAS u32x4*)(lds + L_MISC + 96);
        const unsigned k_ = max(max(max(k0[0], k0[1]), max(k0[2], k0[3])), max(max(k1[0], k1[1]), max(k1[2], k1[3])));
        const u32x2 sp = *(const LAS u32x2*)(lds + L_MISC + 112 + (l8 & 24));
        const unsigned sk = misc[28 + (~k_ & 7)];
        pub = lane < 4 ? u32x4{sp[0], tag, sp[1], tag} : u32x4{k_, tag, sk, tag};
        PIN(pub);
      }
    }
    if ((cgu || cdn) && v == 3) {
      const LAS f32x2* c_ = (const LAS f32x2*)(lds + L_CMB + l8);
      const f32x2 c0 = c_[0], c1 = c_[64], c2 = c_[128], c3 = c_[192];
      float s0 = c0[0] + c1[0], s1 = c2[0] + c3[0]; PIN(s0); PIN(s1); s0 += s1; PIN(s0);
      float s2 = c0[1] + c1[1], s3 = c2[1] + c3[1]; PIN(s2); PIN(s3);
      pub = u32x4{__float_as_uint(s0), tag, __float_as_uint(s2 + s3), tag};
      PIN(pub);
    }
    if (sl >= 19 && sl < NEED_TO && (sl & 1)) { if (v == 0) asm volatile("buffer_load_dword %0, %1, %2, 0 offen sc1" : "+v"(fl) : "v"(fo), "s"(rws) : "memory"); ++ops.y0; }
    constexpr int h0 = 2 + prefix(hsched, sl + 1), hn = hsched[sl + 1];
    static_assert(hn == 0 || h0 + hn <= 2 * (sl + 4), "image not free yet");
#pragma unroll
    for (int n = h0 + (sl == 0 ? PRE_H : 0); n < h0 + hn && sl >= 0; ++n) HALF1(n);
    if (sl == Q0_AT) { QBIG(0); m2[0] = ops; }
    if (sl == Q1_AT) { QBIG(1); m2[1] = ops; }
    if (sl == Q2_AT) { QBIG(2); m2[2] = ops; }
    if (sl == 18) {
      mp2 = ops;
      if (v != 0) { QBIG(3); ops += -8; }
      if (v == 0) {
        STAMP(2);
        unsigned f1, f2;
        int cap = SPIN_CAP;
        asm volatile("buffer_load_dword %0, %4, %5, 0 offen sc1\n\ts_sleep 3\n\tbuffer_load_dword %1, %4, %5, 0 offen sc1\n\ts_sleep 3\n"
                     "1:\n\tbuffer_load_dword %2, %4, %5, 0 offen sc1\n\ts_waitcnt vmcnt(2)\n\tv_cmp_ne_u32 vcc, %6, %0\n\ts_cbranch_vccz 2f\n\t"
                     "buffer_load_dword %0, %4, %5, 0 offen sc1\n\ts_waitcnt vmcnt(2)\n\tv_cmp_ne_u32 vcc, %6, %1\n\ts_cbranch_vccz 2f\n\t"
                     "buffer_load_dword %1, %4, %5, 0 offen sc1\n\ts_waitcnt vmcnt(2)\n\tv_cmp_ne_u32 vcc, %6, %2\n\ts_cbranch_vccz 2f\n\t"
                     "s_sub_u32 %3, %3, 1\n\ts_cmp_lg_u32 %3, 0\n\ts_cbranch_scc1 1b\n2:"
                     : "=&v"(f1), "=&v"(f2), "=&v"(fl), "+s"(cap) : "v"(fo1), "s"(rws), "s"(tag) : "memory", "vcc", "scc");
        STAMP(3);
        PIMG(2, 8, 0); PIMG(2, 8, 1); PIMG(2, 8, 2); PIMG(2, 8, 3);
        asm volatile("buffer_load_dword %0, %3, %4, 0 offen sc1" : "+v"(fl), "+v"(f1), "+v"(f2) : "v"(fo), "s"(rws) : "memory");
        QBIG(3); ops += -8;
        PIMG(3, 2, 0); PIMG(3, 2, 1); PIMG(3, 2, 2); PIMG(3, 2, 3);
        asm volatile("s_waitcnt vmcnt(41)" : "+v"(f1), "+v"(f2) :: "memory");
      }
      ops += 8; ops.y0 += 16; m2[3] = ops;
    }
    if constexpr (sl >= 19 && sl < 29) { QBIG(sl - 15); PIMG(sl - 15, ((sl - 15) % 3) * 3 + 2, v); ops += 4; m2[sl - 15] = ops; }
    if (sl == 29) { QS(0, 6); PS(0, 8); PS(1, 7); }
    if (sl == 30) { QS(1, 0); QS(2, 1); PS(2, 2); }
    if (sl == 31) { QS(3, 3); PS(3, 5); }
    if (sl == SC_AT) { if (v == 3) ST4(pub, amp); else tag = rfl(misc[2]); ++ops.y3; }
    if (cgu && v == 3) ST4(pub, puo);
    if (cdn && v == 3) ST4(pub, pdo);
    if (cgu || cdn) ++ops.y3;
    if (sl == PT_AT) {
      TOUCH(" sc1", rws, pto);
    }
    if (sl == AM_AT) { LD4Z(" sc1", tb, rws, tbo); LD4Z(" sc1", am, rws, amo); mam = ops; }
    if (sl == GG_AT) { GGLD(); ops += 4; mgg = ops; }
    if (sl == XE_AT) {
      const u32x4 k0 = *(const LAS u32x4*)(lds + L_MISC + 16), k1 = *(const LAS u32x4*)(lds + L_MISC + 32);
      key = k0[0]; sg = k0[1];
      if (k0[2] > key) { key = k0[2]; sg = k0[3]; }
      if (k1[0] > key) { key = k1[0]; sg = k1[1]; }
      if (k1[2] > key) { key = k1[2]; sg = k1[3]; }
      idx = min(0xffff - (int)(rfl(key) & 0xffffu), S - 1);
      asm volatile("s_nop 4\n\tbuffer_load_ushort %0, %2, %3, %4 offen\n\tbuffer_load_ushort %1, %2, %3, %4 offen offset:16" : "=&v"(xe), "=&v"(xf)
                   : "v"(xeo), "s"(rx), "s"(idx * D * 2) : "memory");
      ops += 2;
      mxe = ops;
    }
    if (sl == AT_AT + 1) { if (v == 3) ST1(tag, b == 0 && lane == 0 ? (int)WS_EPOCH : OOB); ++ops.y3; }
    if constexpr (usched[sl + 1] > 0) { constexpr int ui = uidx(sl); const int o_ = uo[ui]; TOUCH("", rq, o_); }
    constexpr int tn = tsched[sl + 1], wn = wsched[sl + 1];
#pragma unroll
    for (int k = nw; k < nw + wn; ++k) {
      if (k < 16) ldw(w[k & 15], rgu, vgu, k * F * 2);
      else ldw(wd[(k - 16) & 7], rd, k < 22 ? vdn : vdl, k < 22 ? (((k - 16) >> 1) * 8 + (k & 1)) * D * 2 : 0);
      ++ops; mw[k] = ops;
    }
    work(slc);
    if (sl == SET2_AT) { static_assert(prefix(wsched, 20) <= 16 && CGU_AT > 18, ""); setup2(); }
    if (sl == NBAR - 1) {
      unsigned got = 0;
      bool own = (idx >> 7) == tp1;
#pragma unroll 1
      for (int spins = 0; spins < SPIN_CAP && (got != 7 || own); ++spins) {
        if (own) FINLD();
        asm volatile("buffer_load_dword %0, %1, %2, 0 offen sc1\n\ts_waitcnt vmcnt(0)" : "=&v"(pl) : "v"(co), "s"(rws) : "memory");
        const unsigned g_ = got | ((unsigned)ALLEQ(pl, tag) & 7u);
        if (g_ != got) { got = g_; misc[12 + v] = got; if (got >> (lane & 3) & 1) co = OOB; }
        if (own) {
          PIN4(fin[0], fin[1], fin[2], fin[3]);
          unsigned bad = 0;
#pragma unroll
          for (int k = 0; k < 4; ++k) bad |= (fin[k][1] ^ tag) | (fin[k][3] ^ tag);
          if (__all(bad == 0)) { final_sum(); own = false; }
        }
        __builtin_amdgcn_s_sleep(2);
      }
      STAMP(11);
    }
    constexpr bool wrote = sl == AT_AT || sl == GE_AT || (vsched[sl + 1] > 0 && prefix(vsched, sl + 2) == 8);
    if (wrote) WAIT_LGKM0();
    if (sl == CT2_AT || sl == CT3_AT) {
      if (v == 3) asm volatile("global_load_dword %0, %1, %2" : "+v"(tch) : "v"(min((b >> 3 & 7) + lane * 8, CODE_LINES - 1) * 128), "s"(pc) : "memory");
      ++ops.y3;
    }
#pragma unroll
    for (int n = nt; n < nt + tn; ++n) {
      const int m = n < 112 ? n >> 4 : (n >> 3) - 7, r = n < 112 ? n & 15 : n & 7;
      stouch(sd, (n < 112 ? ga_ : gb_) + KTILE(m) * 128, (r >> 1) * S * 2 + (r & 1) * 128 + 60);
    }
  };
  static_for(slot, std::make_integer_sequence<int, NBAR + 1>{});
  asm volatile("s_waitcnt vmcnt(0) lgkmcnt(0)" : "+s"(sd), "+v"(tch) :: "memory");
  STAMP_OUT();
}

#define LDF(off) (*reinterpret_cast<const bf16x8*>(lds + (off)))
#define SB() __builtin_amdgcn_sched_barrier(0)
__device__ __forceinline__ void mm(f32x4& c, const bf16x8& a, const bf16x8& b, bool zero) {
  if (zero) asm volatile("v_mfma_f32_16x16x32_bf16 %0, %1, %2, 0" : "=&v"(c) : "v"(a), "v"(b));
  else asm volatile("v_mfma_f32_16x16x32_bf16 %0, %1, %2, %0" : "+v"(c) : "v"(a), "v"(b));
}
__device__ __forceinline__ void mm2(f32x4& c, const bf16x8& a, const bf16x8& b, const bf16x8& a2, const bf16x8& b2, bool zero) {
  if (zero) asm volatile("v_mfma_f32_16x16x32_bf16 %0, %1, %2, 0\n\tv_mfma_f32_16x16x32_bf16 %0, %3, %4, %0" : "=&v"(c) : "v"(a), "v"(b), "v"(a2), "v"(b2));
  else asm volatile("v_mfma_f32_16x16x32_bf16 %0, %1, %2, %0\n\tv_mfma_f32_16x16x32_bf16 %0, %3, %4, %0" : "+v"(c) : "v"(a), "v"(b), "v"(a2), "v"(b2));
}
#define MWAIT() do { asm volatile("s_nop 7\n\ts_nop 7"); SB(); } while (0)
#define BLOCK(a, G0, Q0, ZERO, X) do { _Pragma("unroll") for (int n_ = 0; n_ < 16; ++n_) { \
    const int i_ = n_ >> 2, g_ = (i_ ^ n_ >> 1) & 1, h_ = (n_ ^ n_ >> 1) & 1; \
    if (n_ < 6) mm(a[i_][(G0) + g_], p[h_][i_], q[(Q0) + 2 * g_ + h_], (ZERO) && !(n_ & 1)); \
    else if (!(n_ & 1)) mm2(a[i_][(G0) + g_], p[h_][i_], q[(Q0) + 2 * g_ + h_], p[1 - h_][i_], q[(Q0) + 2 * g_ + 1 - h_], ZERO); \
    SB(); X(n_); SB(); } } while (0)
#define LDQ(n, N0, Q0, a0, a1) if ((n) >= (N0) && (n) < (N0) + 4) q[(Q0) + (n) - (N0)] = LDF((((n) - (N0)) & 1 ? (a1) : (a0)) + (((n) - (N0)) >> 1) * 2048)
#define LDP(n, N0, i, a0, a1) if ((n) == (N0) || (n) == (N0) + 1) p[(n) - (N0)][i] = LDF(((n) == (N0) ? (a0) : (a1)))

__device__ __forceinline__ void consumer(int wid, int lane, int b, char* lds, karg_t ka, unsigned tch, int tp1, int tq1 STAMP_ARG) {
  STAMP_DECL;
  int g_cb = L_DBG;
  const rsrc_t rws = mk_rsrc((const void*)ka[9], WS_END), rx = mk_rsrc((const void*)ka[0], S * D * 2), rwr = mk_rsrc((const void*)ka[5], D * 2);
  u64 pout = ka[10];
  asm volatile("" : "+s"(pout));
  const int z = tq1 >> 3, te = tq1 & 7;
  const int wp = wid >> 1, wq = wid & 1, fr = lane & 15, fq = lane >> 4;
  const int c0 = (fq ^ (fr & 7)) * 16, c1 = ((fq + 4) ^ (fr & 7)) * 16;
  const int q0 = (wq * 64 + fr) * 128 + c0, q1 = (wq * 64 + fr) * 128 + c1;
  volatile LAS unsigned* misc = (volatile LAS unsigned*)(lds + L_MISC);
  f32x4 acc[4][4];
#pragma unroll
  for (int i = 0; i < 4; ++i)
#pragma unroll
    for (int j = 0; j < 4; ++j) acc[i][j] = f32x4{0.f, 0.f, 0.f, 0.f};
  bf16x8 p[2][4], q[8];
  unsigned tag = 0;
  u32x4 xv[4];
#define XO(l) (((tp1 * 128 + wq * 64 + ((l) & 15)) * D + te * 128 + z * 64 + wp * 32 + ((l) >> 4) * 8) * 2)
  {
    const int xt = (2 * te + z - KROT(tp1)) & 15;
    const int xrow = wq * 64 + 16 * ((fr >> 2) & 1) + 4 * (fr >> 3) + (fr & 3);
    const int xr = xrow * 128 + (((wp * 4 + fq) ^ (xrow & 7)) * 16);
#define XV(u) do { _Pragma("unroll") for (int k = 0; k < 4; ++k) \
      xv[k] = *reinterpret_cast<const u32x4*>(lds + (u) * UB + xr + (k >> 1) * 4096 + (k & 1) * 1024); } while (0)
    const int ca = wp ? c1 : c0, cb = wp ? c0 : c1;
    int p0 = (wp * 64 + fr) * 128 + ca, p1 = (wp * 64 + fr) * 128 + cb, w0 = (wq * 64 + fr) * 128 + ca, w1 = (wq * 64 + fr) * 128 + cb;
    f32x4 acs[4];
    bf16x8 ones = BF8((u32x4{0x3f803f80u, 0x3f803f80u, 0x3f803f80u, 0x3f803f80u}));
    u32x4 st[4], wv[4];
    unsigned epoch;
    float sw;
    const int row0 = tp1 * 128 + tq1 * 8 + wid * 2;
    char* sc = lds + L_ST + wid * 2048;
    int o1 = WS_AMAX + b * 16, o2 = WS_STAT + row0 * 8;
    asm volatile("" : "+s"(o1), "+s"(o2));
    int eo = lane == 0 ? WS_EPOCH : lane == 1 ? o1 : o2, wo = ((fr & 7) + fq * 8) * 16, vo = (row0 + (fr >> 3)) * (D * 2) + wo;
#pragma unroll
    for (int i = 0; i < 4; ++i) wv[i] = __builtin_bit_cast(u32x4, ones);
    PIN4(wv[0], wv[1], wv[2], wv[3]);
    asm volatile("" : "+v"(eo), "+v"(wo), "+v"(vo) : "s"(rws), "s"(rx), "s"(rwr));
    {
      asm volatile("buffer_load_dword %0, %5, %6, 0 offen sc1\n\t"
                   "s_mov_b64 exec, %8\n\t"
                   "buffer_load_dwordx4 %1, %9, %7, 0 offen\n\tbuffer_load_dwordx4 %2, %9, %7, 0 offen offset:512\n\t"
                   "buffer_load_dwordx4 %3, %9, %7, 0 offen offset:1024\n\tbuffer_load_dwordx4 %4, %9, %7, 0 offen offset:1536\n\t"
                   "s_mov_b64 exec, -1"
                   : "=&v"(epoch), "+&v"(wv[0]), "+&v"(wv[1]), "+&v"(wv[2]), "+&v"(wv[3])
                   : "v"(eo), "s"(rws), "s"(rwr), "s"(0x00ff00ff00ff00ffull), "v"(wo) : "memory");
    }
    auto stats0 = [&]() __attribute__((always_inline)) {
      asm volatile("buffer_load_dwordx4 %0, %4, %5, 0 offen\n\tbuffer_load_dwordx4 %1, %4, %5, 0 offen offset:512\n\t"
                   "buffer_load_dwordx4 %2, %4, %5, 0 offen offset:1024\n\tbuffer_load_dwordx4 %3, %4, %5, 0 offen offset:1536"
                   : "=&v"(st[0]), "=&v"(st[1]), "=&v"(st[2]), "=&v"(st[3]) : "v"(vo), "s"(rx) : "memory");
    };
    auto stats1 = [&]() __attribute__((always_inline)) {
      f32x4 d1 = {0.f, 0.f, 0.f, 0.f}, d2 = d1, d4 = d1;
      asm volatile("s_waitcnt vmcnt(4)" : "+v"(epoch), "+v"(tch), "+v"(wv[0]), "+v"(wv[1]), "+v"(wv[2]), "+v"(wv[3]) :: "memory");
      tag = mk_tag(epoch);
#pragma unroll
      for (int i = 0; i < 4; ++i) d4 = MFMA(BF8(wv[i]), BF8(wv[i]), d4);
      const int s4 = __builtin_bit_cast(int, (d4[0] + d4[1]) + (d4[2] + d4[3]));
      sw = __builtin_bit_cast(float, __builtin_amdgcn_readlane(s4, 8)) + __builtin_bit_cast(float, __builtin_amdgcn_readlane(s4, 24));
      asm volatile("s_waitcnt vmcnt(0)" : "+v"(st[0]), "+v"(st[1]), "+v"(st[2]), "+v"(st[3]), "+v"(sw) :: "memory");
      STAMP(7);
#pragma unroll
      for (int i = 0; i < 4; ++i) { d2 = MFMA(BF8(st[i]), BF8(st[i]), d2); d1 = MFMA(BF8(st[i]), BF8(wv[i]), d1); }
      *reinterpret_cast<f32x4*>(sc + 1024 + lane * 16) = d2;
      *reinterpret_cast<f32x4*>(sc + lane * 16) = d1;
      asm volatile("" ::: "memory");
      STAMP(11);
    };
    float sa, sx, sq, mbv, den;
    auto stats2 = [&](int n) __attribute__((always_inline)) {
      const int r = fr >> 3;
      if (n == 0) {
        const char* ad = sc + (fr >> 2) * 256 + (fr & 3) * 4;
        sa = *reinterpret_cast<const float*>(ad + (fr & 7) * 16); sx = *reinterpret_cast<const float*>(ad + 128); sq = *reinterpret_cast<const float*>(ad + 1024 + fr * 16);
      }
      if (n == 6) { DPP_ADD(sa, 0xb1, 0xf, true); DPP_ADD(sx, 0xb1, 0xf, true); DPP_ADD(sq, 0xb1, 0xf, true); }
      if (n == 7) { DPP_ADD(sa, 0x4e, 0xf, true); DPP_ADD(sx, 0x4e, 0xf, true); DPP_ADD(sq, 0x4e, 0xf, true); }
      if (n == 8) { DPP_ADD(sa, 0x141, 0xf, true); DPP_ADD(sx, 0x141, 0xf, true); DPP_ADD(sq, 0x141, 0xf, true); }
      if (n == 9) { sx *= 1.0f / D; sq = fmaxf(fmaf(-sx, sx, sq * (1.0f / D)), 0.f); }
      if (n == 10) sq = rbf(rbf(sq) + 1e-5f);
      if (n == 11) { mbv = rbf(sx); den = rbf(__builtin_amdgcn_sqrtf(sq)); }
      if (n == 13) sa = (sa - mbv * sw) * __builtin_amdgcn_rcpf(den);
      if (n == 14) {
        const unsigned rb = pack2(sa, sa) & 0xffffu;
        unsigned srt = (rb & 0x8000u) ? (~rb & 0xffffu) : (rb | 0x8000u);
        if (sa != sa) srt = 0u;
        LAS unsigned* mk = (LAS unsigned*)(lds + L_MISC) + wid * 2 + r;
        mk[20] = (srt << 16) | (unsigned)(0xffff - row0 - r); mk[28] = pack2(mbv, den);
        *(LAS unsigned*)(lds + L_MISC + 8) = tag;
      }
      if (n >= 6 && n < 14) asm volatile("" : "+v"(sa), "+v"(sx), "+v"(sq));
    };
#pragma unroll
    for (int i = 0; i < 4; ++i) { acs[i] = f32x4{0.f, 0.f, 0.f, 0.f}; PIN4(acc[i][0], acc[i][1], acc[i][2], acc[i][3]); }
    PIN4(acs[0], acs[1], acs[2], acs[3]); PIN(ones);
    int ph0 = p0 + 4 * UB, ph1 = p1 + 4 * UB, wh0 = w0 + 5 * UB, wh1 = w1 + 5 * UB, wl0 = w0 + UB, wl1 = w1 + UB;
    PIN4(ph0, ph1, wh0, wh1); PIN4(wl0, wl1, p0, p1);
    CBAR();
    STAMP(12);
#define XU(i) ((0x7230u >> ((i) & 3) * 4) & 15)
#define WU(i) ((0x8541u >> ((i) & 3) * 4) & 15)
#define XA(h, i) (XU(i) < 4 ? p##h + XU(i) * UB : ph##h + (XU(i) - 4) * UB)
#define WA(h, i) (WU(i) < 5 ? wl##h + (WU(i) - 1) * UB : wh##h + (WU(i) - 5) * UB)
#define RSUM(j) mm(acs[j], ones, q[2 * (j)], false)
#define X1A(n) LDP(n, 0, 3, XA(0, i1) + 6144, XA(1, i1) + 6144); LDQ(n, 2, 4, WA(0, i1) + 4096, WA(1, i1) + 4096); if ((n) == 7) RSUM(0); if ((n) == 11) RSUM(1)
#define X1B(n) LDQ(n, 0, 0, WA(0, i1 + 1), WA(1, i1 + 1)); LDP(n, 4, 0, XA(0, i1 + 1), XA(1, i1 + 1)); LDP(n, 8, 1, XA(0, i1 + 1) + 2048, XA(1, i1 + 1) + 2048); \
      LDP(n, 12, 2, XA(0, i1 + 1) + 4096, XA(1, i1 + 1) + 4096); if ((n) == 7) RSUM(2); if ((n) == 11) RSUM(3)
#define X1S(n) stats2(n); X1A(n)
#define STEP1(t, i) do { constexpr int i1 = i; \
      if ((t) == 0) { stats0(); SB(); } \
      if ((t) == 2) BLOCK(acc, 0, 0, false, X1S); else BLOCK(acc, 0, 0, false, X1A); \
      if (__builtin_expect((t) == xt, 0)) XV(XU(i)); \
      if ((t) == 1) stats1(); \
      WAIT_LGKM0(); \
      CBAR(); \
      BLOCK(acc, 2, 4, false, X1B); } while (0)
#pragma unroll
    for (int n = 0; n < 4; ++n) q[n] = LDF((n & 1 ? WA(1, 0) : WA(0, 0)) + (n >> 1) * 2048);
#pragma unroll
    for (int i = 0; i < 3; ++i) { p[0][i] = LDF(XA(0, 0) + i * 2048); p[1][i] = LDF(XA(1, 0) + i * 2048); }
    SB();
    STEP1(0, 0); STEP1(1, 1); STEP1(2, 2);
#pragma unroll 1
    for (int t = 3; t < 15; t += 4) { STEP1(t, 3); STEP1(t + 1, 0); STEP1(t + 2, 1); STEP1(t + 3, 2); }
    if (xt == 15) XV(7);
    f32x4 mb[2][2], rd[2][2];
    const float sgn = z ? -1.f : 1.f;
#define TABS(n) rd[(n) >> 1 & 1][(n) & 1] = *reinterpret_cast<const f32x4*>(lds + L_MB + 512 + (wp * 64 + ((n) >> 1) * 32 + fq * 8 + ((n) & 1) * 4) * 4); \
      mb[(n) >> 1 & 1][(n) & 1] = *reinterpret_cast<const f32x4*>(lds + L_MB + (wp * 64 + ((n) >> 1) * 32 + fq * 8 + ((n) & 1) * 4) * 4)
    {
      constexpr int i1 = 3;
#define X1L(n) LDQ(n, 0, 4, WA(0, i1) + 4096, WA(1, i1) + 4096); LDP(n, 4, 3, XA(0, i1) + 6144, XA(1, i1) + 6144); if ((n) == 1) RSUM(0); if ((n) == 3) RSUM(1); \
      if ((n) == 7) RSUM(2); if ((n) == 9) RSUM(3); \
      if ((n) == 12) *reinterpret_cast<f32x4*>(lds + L_CS + wid * 256 + fr * 16) = f32x4{acs[0][0], acs[1][0], acs[2][0], acs[3][0]}; \
      if ((n) == 13) PIN4(acs[0], acs[1], acs[2], acs[3]); \
      if ((n) >= 6 && (n) < 10) { TABS((n) - 6); } if ((n) == 14) { rd[0][0] *= sgn; rd[0][1] *= sgn; rd[1][0] *= sgn; rd[1][1] *= sgn; PIN4(rd[0][0], rd[0][1], rd[1][0], rd[1][1]); }
      BLOCK(acc, 0, 0, false, X1L);
    }
    WAIT_LGKM0();
    CBAR();
    f32x4 cs;
    const int fb = (wq == z ? 4096 : 0) + lane * 16;
    const int ao = WS_ABT + tq1 * ABT_ST + (tp1 * 2 + wp) * UB + fb;
    char* own = lds + wp * SLOT + 2 * UB + fb;
#define FO(m, j) (((j) >> 1) * 8192 + ((j) & 1) * 2048 + (m) * 1024)
#define EPI(m, j) do { const u32x4 o = pack8((mb[m][0] * cs[j] + acc[2 * (m)][j]) * rd[m][0], (mb[m][1] * cs[j] + acc[2 * (m) + 1][j]) * rd[m][1]); \
      __builtin_amdgcn_raw_buffer_store_b128(o, rws, ao + FO(m, j), 0, SC1); *reinterpret_cast<u32x4*>(own + FO(m, j)) = o; } while (0)
    static_assert(AT_AT < 15, "");
#define X1E(n) if ((n) == 0) cs = *reinterpret_cast<const f32x4*>(lds + L_CS + (wid ^ 2) * 256 + fr * 16); \
      if ((n) == 4) { cs += f32x4{acs[0][0], acs[1][0], acs[2][0], acs[3][0]}; PIN(cs); } \
      if ((n) == 5) EPI(0, 0); if ((n) == 7) EPI(0, 1); if ((n) == 9) EPI(1, 0); if ((n) == 11) EPI(1, 1); if ((n) == 13) EPI(0, 2); if ((n) == 15) EPI(0, 3)
    BLOCK(acc, 2, 4, false, X1E);
    MWAIT();
    STAMP(2);
    EPI(1, 2); EPI(1, 3);
  }
  f32x4 acb[4][4];
  const int pp = wp * 8192 + lane * 16;
  bool sent = false;
#define FLAG(off, n) do { if (!sent && vm_now() <= (n)) { sent = true; asm volatile("" ::: "memory"); \
    if (lane == 0) __builtin_amdgcn_raw_buffer_store_b32(tag, rws, off, 0, SC1); STAMP(FST); } } while (0)
#define FST 4
  const int f1o = WS_FLAG1 + (tq1 * 16 + tp1) * 128 + wid * 4;
  int qa[3], qb[3], pq[2];
  qa[0] = q0; qb[0] = q1; qa[1] = q0 + SLOT; qb[1] = q1 + SLOT; qa[2] = q0 + 2 * SLOT; qb[2] = q1 + 2 * SLOT; pq[0] = pp + 2 * UB; pq[1] = pp + 2 * SLOT + 2 * UB;
  PIN4(qa[1], qb[1], qa[2], qb[2]); PIN4(pq[0], pq[1], qa[0], qb[0]);
  WAIT_LGKM0();
  CBAR();
  STAMP(5);
#define PQ(i) ((i) < 2 ? pq[0] + (i) * SLOT : pq[1])
#define X2A(n) LDP(n, 0, 3, PQ(s2) + 6144, PQ(s2) + 7168); LDQ(n, 2, 4, qa[s2] + UB + 4096, qb[s2] + UB + 4096); XX(n)
#define X2B(n) LDQ(n, 0, 0, qa[s2], qb[s2]); XX(n)
#define X2C(n) LDQ(n, 0, 4, qa[s2] + 4096, qb[s2] + 4096); XX(n)
#define X2D(n) LDQ(n, 0, 0, qa[s2] + UB, qb[s2] + UB); LDP(n, 4, 0, PQ(s2), PQ(s2) + 1024); LDP(n, 8, 1, PQ(s2) + 2048, PQ(s2) + 3072); \
    LDP(n, 12, 2, PQ(s2) + 4096, PQ(s2) + 5120); XX(n)
#define HEAD2(sl, ZERO) do { constexpr int s2 = sl; BLOCK(acb, 0, 0, ZERO, X2A); BLOCK(acb, 2, 4, ZERO, X2B); BLOCK(acc, 0, 0, ZERO, X2C); } while (0)
#define TAIL2(sn, ZERO) do { constexpr int s2 = sn; WAIT_LGKM0(); CBAR(); BLOCK(acc, 2, 4, ZERO, X2D); } while (0)
#pragma unroll
  for (int n = 0; n < 4; ++n) q[n] = LDF((n & 1 ? qb[0] : qa[0]) + UB + (n >> 1) * 2048);
#pragma unroll
  for (int i = 0; i < 3; ++i) { p[0][i] = LDF(PQ(0) + i * 2048); p[1][i] = LDF(PQ(0) + i * 2048 + 1024); }
  SB();
#define XX(n) if (((n) & 7) == 7) FLAG(f1o, 0)
  HEAD2(0, true); TAIL2(1, true); HEAD2(1, false);
#undef XX
#define XX(n)
  if (!sent) { wait_vm<0>(); FLAG(f1o, 0); }
  u32x4 gf[4];
#define TM(k, l) (*reinterpret_cast<const float*>(lds + L_MB + (wq * 64 + (k) * 16 + ((l) & 15)) * 4))
#define TR(k, l) (*reinterpret_cast<const float*>(lds + L_MB + 512 + (wq * 64 + (k) * 16 + ((l) & 15)) * 4))
#define GATE(k, l) do { const float tr = TR(k, l), tm = TM(k, l) * tr; _Pragma("unroll") for (int c = 0; c < 4; ++c) { \
      const f32x2 n = f32x2{lo2f(xv[k][c]), hi2f(xv[k][c])} * tr + tm, a = n * -1.44269504f; \
      const f32x2 d = f32x2{__builtin_amdgcn_exp2f(a[0]), __builtin_amdgcn_exp2f(a[1])} + 1.0f; \
      const f32x2 g = n * f32x2{__builtin_amdgcn_rcpf(d[0]), __builtin_amdgcn_rcpf(d[1])}; \
      gf[k][c] = pack2(g[0], g[1]); } PIN(gf[k]); PIN(xv[k]); } while (0)
  {
    const int lg = lane_now();
#pragma unroll
    for (int k = 0; k < GF_N; ++k) GATE(k, lg);
  }
  __builtin_amdgcn_sched_barrier(0);
  STAMP(1);
#pragma unroll 1
  for (int n = 0; n < 4; ++n) { TAIL2(2, false); HEAD2(2, false); TAIL2(0, false); HEAD2(0, false); TAIL2(1, false); HEAD2(1, false); }
#define PU(k) ((k) == 0 ? 8 : (k) == 1 ? 7 : (k) == 2 ? 2 : 5)
#define QU(k) ((k) == 0 ? 6 : (k) == 1 ? 0 : (k) == 2 ? 1 : 3)
  const int ln = lane_now();
  const int xw = WS_XCH + wid * XW + ln * 16;
  int p7 = pq[1] - UB;
  PIN(p7);
#define PAD(k) (PU(k) >= 7 ? p7 + (PU(k) - 7) * UB : pq[0] + (PU(k) - 2) * UB)
#define QA0(k) (qa[QU(k) / 3] + QU(k) % 3 * UB)
#define QA1(k) (qb[QU(k) / 3] + QU(k) % 3 * UB)
#define SEND(p, a0, a1, j) __builtin_amdgcn_raw_buffer_store_b128(pack8(a0[j], a1[j]), rws, xw + b * XB + (p) * 4096 + (j) * 1024, 0, SC1)
#define JOB(K, I0, PI, KN, IN, FIRST, EXTRA) do { _Pragma("unroll") for (int n_ = 0; n_ < 16; ++n_) { \
    const int g_ = n_ >> 2, i_ = (g_ ^ n_ >> 1) & 1, h_ = (n_ ^ n_ >> 1) & 1; \
    mm(acc[(I0) + i_][g_], p[h_][(PI) + i_], q[2 * g_ + h_], false); SB(); \
    if (FIRST) { LDQ(n_, 0, 4, QA0(K) + 4096, QA1(K) + 4096); } else if (n_ < 2) q[6 + n_] = LDF((n_ ? QA1(K) : QA0(K)) + 6144); \
    if ((KN) >= 0) { \
      if (n_ >= 4 && (n_ & 3) < 2) q[2 * g_ - 2 + (n_ & 1)] = LDF((n_ & 1 ? QA1(KN) : QA0(KN)) + (g_ - 1) * 2048); \
      LDP(n_, 6, 2 - (PI), PAD(KN) + (IN) * 2048, PAD(KN) + (IN) * 2048 + 1024); LDP(n_, 10, 3 - (PI), PAD(KN) + (IN) * 2048 + 2048, PAD(KN) + (IN) * 2048 + 3072); } \
    if (n_ == 8 && (K) + 2 * (I0) >= RX_FROM) mw = misc[12 + wid]; \
    EXTRA(n_); SB(); } } while (0)
#define X_NONE(n)
#define X_S0(n) if (((n) & 3) == 2) SEND(0, acb[2], acb[3], (n) >> 2)
#define X_S1(n) if (((n) & 3) == 2) SEND(1, acb[0], acb[1], (n) >> 2)
  unsigned chk;
#define X_S2(n) if ((n) < 8 && ((n) & 1)) { const u32x4 o_ = pack8(acc[0][(n) >> 1], acc[1][(n) >> 1]); \
      __builtin_amdgcn_raw_buffer_store_b128(o_, rws, xw + b * XB + 8192 + ((n) >> 1) * 1024, 0, SC1); \
      chk = ((n) == 1 ? tag : chk) + o_[0] + o_[1]; chk += o_[2] + o_[3]; PIN(chk); } \
    if ((n) == 9) __builtin_amdgcn_raw_buffer_store_b64(u32x2{chk, tag}, rws, xw + b * XB + 12288 - ln * 8, 0, SC1)
#define RECV2() do { RECV(e2, 2, 4); asm volatile("buffer_load_dwordx2 %0, %1, %2, 0 offen sc1" \
                 : "+&v"(e5) : "v"(xw + (b ^ 4) * XB + 12288 - ln * 8), "s"(rws) : "memory"); nv += 5; } while (0)
#define FLAGX() do { if (fx < 2 && vm_now() <= nv + (fx ? 0 : 4)) { asm volatile("" ::: "memory"); \
    if (ln == 0) __builtin_amdgcn_raw_buffer_store_b32(tag, rws, xw + b * XB + 13824 + fx * 128, 0, SC1); ++fx; ++nv; } } while (0)
  u32x4 e0[4], e1[4], e2[4]; u32x2 e5;
  unsigned fx = 0, nv = 0;
#define RECV(v, p, pb) asm volatile("buffer_load_dwordx4 %0, %4, %5, 0 offen sc1\n\tbuffer_load_dwordx4 %1, %4, %5, 0 offen offset:1024 sc1\n\t" \
    "buffer_load_dwordx4 %2, %4, %5, 0 offen offset:2048 sc1\n\tbuffer_load_dwordx4 %3, %4, %5, 0 offen offset:3072 sc1" \
    : "+&v"(v[0]), "+&v"(v[1]), "+&v"(v[2]), "+&v"(v[3]) : "v"(xw + (b ^ (pb)) * XB + (p) * 4096), "s"(rws) : "memory")
  unsigned mw, got = 0;
#define RX(n) do { FLAGX(); if ((n) < RX_FROM) break; unsigned m_ = rfl(mw) & ~got; if ((n) < 7) m_ &= -m_; \
    if (m_) { got |= m_; if (m_ & 1) { RECV(e0, 0, 2); nv += 4; } if (m_ & 2) { RECV(e1, 1, 6); nv += 4; } if (m_ & 4) RECV2(); } } while (0)
#define X3D(n) LDQ(n, 0, 0, QA0(0), QA1(0)); LDP(n, 4, 0, PAD(0), PAD(0) + 1024); LDP(n, 8, 1, PAD(0) + 2048, PAD(0) + 3072)
  WAIT_LGKM0();
  CBAR();
  BLOCK(acc, 2, 4, false, X3D);
  STAMP(10);
  CBAR();
  JOB(0, 0, 0, 1, 0, true, X_S0);
  JOB(1, 0, 2, 2, 0, false, X_S1);
  FLAGX();
  CBAR();
  JOB(2, 0, 0, 3, 0, false, X_NONE);
  FLAGX();
  JOB(3, 0, 2, 0, 2, false, X_NONE);
  RX(3);
  STAMP(15);
  JOB(0, 2, 0, 1, 2, false, X_S2);
  nv += 5;
  RX(4);
  JOB(1, 2, 2, 2, 2, false, X_NONE);
  RX(5);
  JOB(2, 2, 0, 3, 2, false, X_NONE);
  RX(6);
  JOB(3, 2, 2, -1, 0, false, X_NONE);
  MWAIT();
  STAMP(6);
  RX(7);
#pragma unroll
  for (int k = GF_N; k < 4; ++k) { mw = misc[12 + wid]; GATE(k, ln); RX(8); }
  {
    const u32x4 k0 = *(const LAS u32x4*)(lds + L_MISC + 16), k1 = *(const LAS u32x4*)(lds + L_MISC + 32);
    const int idx = min(0xffff - (int)(rfl(max(max(k0[0], k0[2]), max(k1[0], k1[2]))) & 0xffffu), S - 1);
    if ((idx >> 6) == tp1 * 2 + wq) {
#pragma unroll 1
      for (int spins = 0; spins < SPIN_CAP; ++spins) { if (rfl(misc[16 + wp * 2] & misc[17 + wp * 2])) break; __builtin_amdgcn_s_sleep(2); }
      asm volatile("" ::: "memory");
      const LAS float* upd = (const LAS float*)(lds + L_UPD) + wp * 32 + (ln >> 4) * 8;
#pragma unroll
      for (int k = 0; k < 4; ++k) {
        if (((idx >> 4) & 3) == k && (idx & 15) == (ln & 15)) {
#pragma unroll
          for (int c = 0; c < 4; ++c) {
            const unsigned n = ln_pair(xv[k][c], TM(k, ln), TR(k, ln));
            gf[k][c] = silu_pair(pack2(lo2f(n) + upd[2 * c], hi2f(n) + upd[2 * c + 1]));
          }
        }
      }
    }
  }
#pragma unroll 1
  for (int spins = 0; spins < SPIN_CAP && (got != 7 || fx < 2); ++spins) { mw = misc[12 + wid]; RX(8); }
  STAMP(9);
  asm volatile("s_waitcnt vmcnt(0)" : "+v"(e0[0]), "+v"(e0[1]), "+v"(e0[2]), "+v"(e0[3]), "+v"(e1[0]), "+v"(e1[1]), "+v"(e1[2]), "+v"(e1[3]),
               "+v"(e2[0]), "+v"(e2[1]), "+v"(e2[2]), "+v"(e2[3]), "+v"(e5) :: "memory");
#define CHK2() __all((((tag + e2[0][0] + e2[0][1]) + (e2[0][2] + e2[0][3] + e2[1][0]) + (e2[1][1] + e2[1][2] + e2[1][3]) + (e2[2][0] + e2[2][1] + e2[2][2]) + \
                       (e2[2][3] + e2[3][0] + e2[3][1]) + (e2[3][2] + e2[3][3])) ^ e5[0] | e5[1] ^ tag) == 0)
  if (__builtin_expect(!CHK2(), 0)) {
#pragma unroll 1
    for (int spins = 0; spins < SPIN_CAP; ++spins) {
      asm volatile("buffer_load_dwordx4 %0, %5, %7, 0 offen sc1\n\tbuffer_load_dwordx4 %1, %5, %7, 0 offen offset:1024 sc1\n\t"
                   "buffer_load_dwordx4 %2, %5, %7, 0 offen offset:2048 sc1\n\tbuffer_load_dwordx4 %3, %5, %7, 0 offen offset:3072 sc1\n\t"
                   "buffer_load_dwordx2 %4, %6, %7, 0 offen sc1\n\ts_waitcnt vmcnt(0)"
                   : "=&v"(e2[0]), "=&v"(e2[1]), "=&v"(e2[2]), "=&v"(e2[3]), "=&v"(e5)
                   : "v"(xw + (b ^ 4) * XB + 8192), "v"(xw + (b ^ 4) * XB + 12288 - ln * 8), "s"(rws) : "memory");
      if (CHK2()) break;
    }
  }
  STAMP(3);
  const rsrc_t rout = mk_rsrc((const void*)pout, S * D * 2);
#pragma unroll
  for (int k = 0; k < 4; ++k) {
    u32x4 ov;
#pragma unroll
    for (int c = 0; c < 4; ++c) {
      const unsigned a1 = e0[k][c], a2 = e1[k][c], a3 = e2[k][c];
      const f32x2 sv = (f32x2{acc[2 + (c >> 1)][k][(c & 1) * 2], acc[2 + (c >> 1)][k][(c & 1) * 2 + 1]} + f32x2{lo2f(a1), hi2f(a1)}) +
                       (f32x2{lo2f(a2), hi2f(a2)} + f32x2{lo2f(a3), hi2f(a3)});
      const f32x2 r = sv * f32x2{lo2f(gf[k][c]), hi2f(gf[k][c])} + f32x2{lo2f(xv[k][c]), hi2f(xv[k][c])};
      ov[c] = pack2(r[0], r[1]);
    }
    __builtin_amdgcn_raw_buffer_store_b128(ov, rout, XO(ln) + k * 16 * D * 2, 0, SC1);
  }
  STAMP(8);
  STAMP_OUT();
}

__global__ __launch_bounds__(512, 2) void fused(const u16* __restrict__ x, const u16* __restrict__ sre, const u16* __restrict__ sim,
                                                const u16* __restrict__ hre, const u16* __restrict__ him, const u16* __restrict__ wr,
                                                const u16* __restrict__ wg, const u16* __restrict__ wu, const u16* __restrict__ wdn,
                                                char* ws, u16* out) {
  extern __shared__ __attribute__((aligned(16))) char lds[];
  const karg_t ka = (karg_t)__builtin_amdgcn_kernarg_segment_ptr();
  u64 px, pre, pim;
  asm volatile("s_load_dwordx2 %0, %3, 0\n\ts_load_dwordx2 %1, %3, 24\n\ts_load_dwordx2 %2, %3, 32" : "=&s"(px), "=&s"(pre), "=&s"(pim) : "s"(ka));
  __builtin_amdgcn_sched_barrier(0);
  const int wid = __builtin_amdgcn_readfirstlane(threadIdx.x >> 6), lane = threadIdx.x & 63;
  const int b = blockIdx.x, g = b & 7, i = b >> 3;
  const int tp1 = (g & 3) * 4 + (i & 3), tq1 = (g >> 2) * 8 + (i >> 2);
  unsigned tch = 0;
  u64 pc;
  asm volatile("s_getpc_b64 %0" : "=s"(pc));
  pc &= ~255ull;
  if (wid == 3) {
    asm volatile("global_load_dword %0, %1, %2" : "+v"(tch) : "v"(min((i & 7) + lane * 8, CODE_LINES - 1) * 128), "s"(pc) : "memory");
  }
  if (__builtin_expect(wid >= 4, 1)) service(wid & 3, lane, b, lds, ka, px, pre, pim, pc, tp1, tq1 STAMP_PASS);
  else {
    asm volatile("s_waitcnt lgkmcnt(0)" :: "s"(px), "s"(pre), "s"(pim));
    consumer(wid, lane, b, lds, ka, tch, tp1, tq1 STAMP_PASS);
  }
}

extern "C" void kernel_launch(void* const* d_in, const int* in_sizes, int n_in, void* d_out, int out_size,
                              void* d_ws, size_t ws_size, hipStream_t stream) {
  (void)hipFuncSetAttribute((const void*)fused, hipFuncAttributeMaxDynamicSharedMemorySize, LDS_BYTES);
  fused<<<256, 512, LDS_BYTES, stream>>>((const u16*)d_in[0], (const u16*)d_in[1], (const u16*)d_in[2], (const u16*)d_in[3],
                                         (const u16*)d_in[4], (const u16*)d_in[6], (const u16*)d_in[7], (const u16*)d_in[8],
                                         (const u16*)d_in[9], (char*)d_ws, (u16*)d_out);
}
```
